```python
import torch

def _softmax(x, axis=-1):
    return torch.softmax(x, dim=axis)
def _gelu(x):
    return torch.nn.functional.gelu(x, approximate="tanh")
def _silu(x):
    return torch.nn.functional.silu(x)
def _sigmoid(x):
    return torch.sigmoid(x)
def _layernorm(x, axis=-1, eps=1e-5):
    m = x.mean(dim=axis, keepdim=True)
    v = x.var(dim=axis, keepdim=True, unbiased=False)
    return (x - m) / torch.sqrt(v + eps)
def _scatter_add(tgt, idx, src, axis=0):
    return tgt.clone().index_add_(axis, idx, src)
def _segment_sum(src, seg, n):
    out = torch.zeros((n,) + src.shape[1:], dtype=src.dtype)
    return out.index_add_(0, seg, src)
def _pad(x, pw):
    flat = tuple(v for pair in reversed(pw) for v in pair)
    return torch.nn.functional.pad(x, flat)

kernel_name = 'sequential_preLN__softmax_full_mha__dense_activation_relu__prompt'

def setup_inputs():

    _w = (lambda a: (lambda t: t.to(torch.bfloat16) if t.is_floating_point() else t)(torch.as_tensor(a)))
    def _A_setup():
        B, S, D = 1, 2048, 640
        return {
            "x": _w((lambda *s: torch.randn(*s, dtype=torch.bfloat16) * 0.05)(B, S, D)),
            "wqkv": _w((lambda *s: torch.randn(*s, dtype=torch.bfloat16) * 0.05)(D, 3 * D)),
            "wo": _w((lambda *s: torch.randn(*s, dtype=torch.bfloat16) * 0.05)(D, D)),
        }
    _A_in = _A_setup()

    def _B_setup():
        B, S, D, Dff = 1, 2048, 640, 1728
        return {
            "x": _w((lambda *s: torch.randn(*s, dtype=torch.bfloat16) * 0.05)(B, S, D)),
            "w1": _w((lambda *s: torch.randn(*s, dtype=torch.bfloat16) * 0.05)(D, Dff)),
            "w2": _w((lambda *s: torch.randn(*s, dtype=torch.bfloat16) * 0.05)(Dff, D)),
        }
    _B_in = _B_setup()

    return {
        "A_x": _A_in["x"],
        "A_wqkv": _A_in["wqkv"],
        "A_wo": _A_in["wo"],
        "B_x": _B_in["x"],
        "B_w1": _B_in["w1"],
        "B_w2": _B_in["w2"],
    }

def reference(A_x, A_wqkv, A_wo, B_x, B_w1, B_w2):
    def _A_ref(x, wqkv, wo):
        B, S, D = x.shape
        H = 32; Dh = D // H
        qkv = (lambda x, *d: x.permute(*d))((x @ wqkv).reshape(B, S, 3, H, Dh), 2, 0, 3, 1, 4)
        q, k, v = qkv[0], qkv[1], qkv[2]
        scale = Dh ** -0.5
        scores = torch.einsum("bhqd,bhkd->bhqk", q, k) * scale
        p = _softmax(scores, axis=-1)
        o = torch.einsum("bhqk,bhkd->bhqd", p, v)
        return (lambda x, *d: x.permute(*d))(o, 0, 2, 1, 3).reshape(B, S, D) @ wo

    def _B_ref(x, w1, w2):
        h = torch.einsum("bsd,df->bsf", x, w1)
        zero = torch.zeros_like(h)
        h = torch.maximum(h, zero)
        return torch.einsum("bsf,fd->bsd", h, w2)

    x = A_x
    h = x + _A_ref(_layernorm(x, axis=-1), A_wqkv, A_wo)
    h2 = h + _B_ref(_layernorm(h, axis=-1), B_w1, B_w2)
    return h2
```

```cpp
#include <hip/hip_runtime.h>
#include <hip/hip_bf16.h>
#include <utility>

typedef float f32x2 __attribute__((ext_vector_type(2)));
typedef float f32x4 __attribute__((ext_vector_type(4)));
typedef float f32x16 __attribute__((ext_vector_type(16)));
typedef float f32x32 __attribute__((ext_vector_type(32)));
typedef short s16x4 __attribute__((ext_vector_type(4)));
typedef short s16x8 __attribute__((ext_vector_type(8)));
typedef __bf16 b16x2 __attribute__((ext_vector_type(2)));
typedef __bf16 b16x8 __attribute__((ext_vector_type(8)));
typedef unsigned u32x2 __attribute__((ext_vector_type(2)));
typedef unsigned u32x4 __attribute__((ext_vector_type(4)));
typedef unsigned long long u64;
#define LAS __attribute__((address_space(3)))
#define GAS __attribute__((address_space(1)))
#define DI __device__ __forceinline__
#define SBAR() __builtin_amdgcn_sched_barrier(0)
typedef __amdgpu_buffer_rsrc_t rsrc_t;

constexpr int SEQ = 2048, DM = 640, DFF = 1728;
constexpr float LN_EPS = 1e-5f;
constexpr float QSCALE = 0.22360679774997896f * 1.4426950408889634f;
constexpr int SC1 = 16;
__host__ __device__ constexpr int uperm(int k) {
  return k + 8 * (int)((0x1004ull >> k) & 1) - 8 * (int)((0x100400ull >> k) & 1) + 6 * (int)((0x10040000000ull >> k) & 1) - 6 * (int)((0x401000000000ull >> k) & 1); }
#define CODE_PF 54336
#define PAGE_PF 0
#define P1STAT 0
#define WIN3 6
#define WIN4 5
#define WIN5 5
#define LATE_W 1
#define STAG 1
#define W2_DEPTH 12
#define W2_A 16
#define W2_B 14
#define W2_C 24
#define WMAX4 10
#define WMAX5 12
#define W2_D 70
#define NOW1E 35
#define W1_PRE 8
#define SIDE_WAIT 4
#define TOUCH 1
#define NW2T 216
#define NCS 14
#define P1_AHEAD 1
#define NW 44
#define N1 0
#define NH 0
#define N2 44
#define NU 58
static_assert(N1 <= NH && NH <= N2 && N2 >= 40 && N2 <= NW && NU - NW <= 14 && NU >= 50 && NU >= N2 && NU <= 70, "W2 schedule");
#define TOUCHQ 1
#define STATK 1
#define PD1 5
#define PD2 12
#define PD3 7
#define PD4 21
#define CSF 0
#define QSTR 256
#define QSL 0
#define WS_AT 0
#define FSTR 256
constexpr unsigned WS_EPOCH = 0;
constexpr unsigned WS_FLAG = 4096;
constexpr int F_QKV = 0, F_O = 256, F_H = F_O + 64 * 32, F_U = F_H + 64 * 16;
constexpr unsigned WS_FW2 = WS_FLAG + 4352 * (FSTR > 256 ? FSTR : 256);
constexpr unsigned WS_CS = WS_FW2 + 256 * 256;
constexpr unsigned WS_XS = WS_CS + 8192;
constexpr unsigned WS_ST = WS_XS + 32768;
constexpr unsigned WS_Q = WS_ST + SEQ * 64;
constexpr unsigned WS_KV = WS_Q + 32 * 81920;
constexpr unsigned WS_O = WS_KV + 32 * 163840;
constexpr unsigned WS_H = WS_O + SEQ * DM * 2;
constexpr unsigned WS_U = WS_H + SEQ * DM * 2;
constexpr unsigned WS_W2 = WS_U + SEQ * DFF * 2;
constexpr unsigned WS_END = WS_W2 + DFF * DM * 2;
constexpr int L_CONST = 61440, L_PROG = L_CONST + 4864, L_XCH = 40960, L_XCH2 = 66560, L_FLAG = 30720, L_DUMMY = 70656, L_WO = 71680, L_WO2 = 20480, L_W2S = 153344, L_W1 = 20480, L_P1ST = 0, LDS_BYTES = 163840;

DI rsrc_t mkrs(const void* p, unsigned bytes) { return __builtin_amdgcn_make_buffer_rsrc((void*)p, 0, bytes, 0x00020000); }
DI void dma16(rsrc_t rs, unsigned lds, unsigned voff, unsigned soff) {
  asm volatile("s_mov_b32 m0, %0\n\ts_nop 0\n\tbuffer_load_dwordx4 %1, %2, %3 offen lds" :: "s"(lds), "v"(voff), "s"(rs), "s"(soff) : "memory"); }
DI void dma16c(rsrc_t rs, unsigned lds, unsigned voff, unsigned soff) {
  asm volatile("s_mov_b32 m0, %0\n\ts_nop 0\n\tbuffer_load_dwordx4 %1, %2, %3 offen sc1 lds" :: "s"(lds), "v"(voff), "s"(rs), "s"(soff) : "memory"); }
template <int C> DI float dppadd(float v) { return v + __uint_as_float(__builtin_amdgcn_update_dpp(0u, __float_as_uint(v), C, 0xf, 0xf, true)); }
DI float rowsum16(float v) { v = dppadd<0xB1>(v); v = dppadd<0x4E>(v); v = dppadd<0x141>(v); return dppadd<0x140>(v); }
DI s16x8 ldsr128(LAS const char* p) { return *(LAS const s16x8*)p; }
DI s16x4 ldstr(LAS const char* p) { return __builtin_amdgcn_ds_read_tr16_b64_v4i16((LAS s16x4*)p); }
DI s16x8 cat(s16x4 a, s16x4 b) { return __builtin_shufflevector(a, b, 0, 1, 2, 3, 4, 5, 6, 7); }
DI s16x4 lo4(s16x8 a) { return __builtin_shufflevector(a, a, 0, 1, 2, 3); }
DI s16x4 hi4(s16x8 a) { return __builtin_shufflevector(a, a, 4, 5, 6, 7); }
DI f32x16 mfma32(s16x8 a, s16x8 b, f32x16 c) { return __builtin_amdgcn_mfma_f32_32x32x16_bf16(__builtin_bit_cast(b16x8, a), __builtin_bit_cast(b16x8, b), c, 0, 0, 0); }
DI f32x4 mfma16(s16x8 a, s16x8 b, f32x4 c) { return __builtin_amdgcn_mfma_f32_16x16x32_bf16(__builtin_bit_cast(b16x8, a), __builtin_bit_cast(b16x8, b), c, 0, 0, 0); }
DI void mfma16i(f32x4& c, s16x8 a, s16x8 b) { asm volatile("v_mfma_f32_16x16x32_bf16 %0, %1, %2, %0" : "+v"(c) : "v"(a), "v"(b)); }
DI void mfma32i(f32x16& c, s16x8 a, s16x8 b) { asm volatile("v_mfma_f32_32x32x16_bf16 %0, %1, %2, %0" : "+v"(c) : "v"(a), "v"(b)); }
DI void mfma4i(f32x4& c, s16x4 a, s16x4 b) { asm volatile("v_mfma_f32_4x4x4_16b_bf16 %0, %1, %2, %0" : "+v"(c) : "v"(a), "v"(b)); }
#define LGKM(n) __builtin_amdgcn_s_waitcnt(0xC07F | ((n) << 8))
template <int V, int N> DI void wait_vl() { __builtin_amdgcn_s_waitcnt(0x0070 | (V & 15) | ((V >> 4) << 14) | (N << 8)); }
DI LAS const char* opaque(LAS const char* p) { asm volatile("" : "+v"(p)); return p; }
#define MFMA_DONE() do { SBAR(); asm volatile("s_nop 7" ::: "memory"); SBAR(); } while (0)
DI f32x4 mfma4(s16x4 a, s16x4 b, f32x4 c) { return __builtin_amdgcn_mfma_f32_4x4x4bf16_1k(a, b, c, 0, 0, 0); }
DI unsigned pk(float lo, float hi) { f32x2 v = {lo, hi}; return __builtin_bit_cast(unsigned, __builtin_convertvector(v, b16x2)); }
DI unsigned relu2(unsigned p) { unsigned r; asm("v_pk_max_i16 %0, %1, 0" : "=v"(r) : "v"(p)); return r; }
DI float bflo(unsigned u) { return __uint_as_float(u << 16); }
DI float bfhi(unsigned u) { return __uint_as_float(u & 0xffff0000u); }
DI float sum32(float v) { auto r = __builtin_amdgcn_permlane32_swap(__float_as_uint(v), __float_as_uint(v), false, false); return __uint_as_float(r[0]) + __uint_as_float(r[1]); }
DI float sum16(float v) { auto r = __builtin_amdgcn_permlane16_swap(__float_as_uint(v), __float_as_uint(v), false, false); return __uint_as_float(r[0]) + __uint_as_float(r[1]); }
DI float max32(float v) { auto r = __builtin_amdgcn_permlane32_swap(__float_as_uint(v), __float_as_uint(v), false, false); return fmaxf(__uint_as_float(r[0]), __uint_as_float(r[1])); }
DI float upper32(float v) { auto r = __builtin_amdgcn_permlane32_swap(__float_as_uint(v), __float_as_uint(v), false, false); return __uint_as_float(r[1]); }
DI s16x8 ldfrag(rsrc_t rs, unsigned voff, unsigned soff) { return __builtin_bit_cast(s16x8, __builtin_amdgcn_raw_buffer_load_b128(rs, voff, soff, SC1)); }
#define BAR() asm volatile("s_barrier" ::: "memory")
#define WAITV_(n) asm volatile("s_waitcnt vmcnt(" #n ")" ::: "memory")
#define WAITV(n) WAITV_(n)
#define WAITV_BAR(n) asm volatile("s_waitcnt vmcnt(" #n ")\n\ts_barrier" ::: "memory")
#define WAITVL_BAR(n) asm volatile("s_waitcnt vmcnt(" #n ") lgkmcnt(0)\n\ts_barrier" ::: "memory")
#define WAITL_BAR() asm volatile("s_waitcnt lgkmcnt(0)\n\ts_barrier" ::: "memory")
#define WAITA_BAR() asm volatile("s_waitcnt vmcnt(0) lgkmcnt(0)\n\ts_barrier" ::: "memory")

#define STAMP(i)
#define STAMP_ARG
#define STAMP_PASS

DI int lane_again() { int l; asm volatile("v_mbcnt_lo_u32_b32 %0, -1, 0\n\tv_mbcnt_hi_u32_b32 %0, -1, %0" : "=v"(l)); return l; }
DI u64 mktag(u64 epoch) { asm volatile("" : "+v"(epoch));
  return ((u64)(unsigned)__builtin_amdgcn_readfirstlane((unsigned)(epoch >> 32)) << 32 | (unsigned)__builtin_amdgcn_readfirstlane((unsigned)epoch)) + 1; }
DI void publish(char* ws, int idx, u64 tag) { __hip_atomic_store((GAS u64*)(ws + WS_FLAG + idx * FSTR), tag, __ATOMIC_RELAXED, __HIP_MEMORY_SCOPE_AGENT); }
template <int N, int STR = FSTR, unsigned BASE = WS_FLAG, int SL = 0> DI void await(const char* ws, const int (&idx)[N], u64 tag) {
  for (int spin = 0; spin < (1 << 17); ++spin) {
    bool ok = true;
#pragma unroll
    for (int k = 0; k < N; ++k) ok &= __hip_atomic_load((GAS u64*)(ws + BASE + idx[k] * STR), __ATOMIC_RELAXED, __HIP_MEMORY_SCOPE_AGENT) == tag;
    if (__all(ok)) break;
    if (SL) __builtin_amdgcn_s_sleep(SL);
  }
}

struct RowLoad { u32x4 xv[5]; u32x2 ep; };
DI RowLoad stats_load(const char* x, char* ws, int mb, int nb, int w, int l) {
  const int sr = w * 4 + (l >> 4), srow = nb * 9 + sr; const bool ok = sr < 9 && srow < 128;
  const rsrc_t rx = mkrs(x, SEQ * DM * 2);
  RowLoad r;
  r.ep = __builtin_amdgcn_raw_buffer_load_b64(mkrs(ws, WS_END), l == 63 ? WS_FLAG + (nb * 16 + mb) * QSTR : WS_EPOCH, 0, SC1);
#pragma unroll
  for (int k = 0; k < 5; ++k) r.xv[k] = __builtin_amdgcn_raw_buffer_load_b128(rx, ok ? (mb * 128 + srow) * 1280 + (l & 15) * 16 + k * 256 : 0x7fff0000u, 0, 0);
  return r;
}
DI u64 stats_send(const RowLoad& r, char* ws, int mb, int nb, int w, int l) {
  const int sr = w * 4 + (l >> 4), srow = nb * 9 + sr; const bool ok = sr < 9 && srow < 128;
  const u64 tag = mktag((u64)r.ep[1] << 32 | r.ep[0]);
  float s1 = 0.f, s2 = 0.f;
#pragma unroll
  for (int k = 0; k < 5; ++k)
#pragma unroll
    for (int e = 0; e < 4; ++e) { const unsigned u = r.xv[k][e]; const b16x2 v = __builtin_bit_cast(b16x2, u);
      s1 = __builtin_amdgcn_fdot2_f32_bf16(v, __builtin_bit_cast(b16x2, 0x3F803F80u), s1, false); s2 = __builtin_amdgcn_fdot2_f32_bf16(v, v, s2, false); }
  s1 = rowsum16(s1); s2 = rowsum16(s2);
  const float mu = s1 * (1.f / DM), rsd = __builtin_amdgcn_rsqf(fmaxf(s2 * (1.f / DM) - mu * mu, 0.f) + LN_EPS);
  __builtin_amdgcn_raw_buffer_store_b64((u32x2){__float_as_uint((l & 1) ? rsd : mu), (unsigned)tag}, mkrs(ws, WS_END), ok && (l & 15) < 2 ? WS_XS + (mb * 128 + srow) * 16 + (l & 1) * 8 : 0x7fff0000u, 0, SC1);
  return tag;
}
template <bool VT> DI u64 p1_consumer(const RowLoad& rl, char* ws, LAS char* smem, int mb, int nb, int w, int l STAMP_ARG) {
  u64 tag;
  const int wm = w >> 1, wn = w & 1, g = l >> 4, r = l & 15;
  f32x4 acc[4][4], cs4[2];
#pragma unroll
  for (int z = 0; z < 16; ++z) { acc[z >> 2][z & 3] = (f32x4){}; asm volatile("" : "+v"(acc[z >> 2][z & 3])); }
#pragma unroll
  for (int z = 0; z < 2; ++z) { cs4[z] = (f32x4){}; asm volatile("" : "+v"(cs4[z])); }
  s16x4 ones = {0x3F80, 0x3F80, 0x3F80, 0x3F80}; asm volatile("" : "+v"(ones));
  const int row = wm * 64 + r, a0 = row * 128 + ((g ^ ((row >> 1) & 7)) << 4);
  const int q = r >> 2, p = l & 3;
  const int wb0 = 16384 + (8 * g + q) * 256 + (((8 * wn + 4 * wm + (p >> 1)) ^ ((q << 2) | ((g & 1) << 1))) << 4) + (p & 1) * 8;
  LAS const char* ab[3][2]; LAS const char* wb[3][4];
#pragma unroll
  for (int z = 0; z < 3; ++z) {
#pragma unroll
    for (int s = 0; s < 2; ++s) ab[z][s] = opaque(smem + z * 65536 + (a0 ^ (s << 6)));
#pragma unroll
    for (int j = 0; j < 4; ++j) wb[z][j] = opaque(smem + z * 65536 + (wb0 ^ (j << 5)));
  }
  s16x8 a[2][4]; s16x4 b[2][4][2];
  auto rd = [&](int k, int e) {
    const int slot = (k >> 1) % 5, z = slot >> 1, o = (slot & 1) * 32768, s = k & 1, n = k & 1;
    if (e < 4) a[n][e] = ldsr128(ab[z][s] + o + e * 2048);
    else b[n][(e - 4) >> 1][e & 1] = ldstr(wb[z][(e - 4) >> 1] + o + s * 8192 + (e & 1) * 1024);
    SBAR();
  };
  BAR();
#pragma unroll
  for (int e = 0; e < 12; ++e) rd(0, e);
  STAMP(1);
#pragma unroll
  for (int k = 0; k < 20; ++k) {
    const int c = k & 1; const bool more = k < 19;
    if (k == STATK) { tag = stats_send(rl, ws, mb, nb, w, l); SBAR(); }
    if ((k & 1) && more) BAR();
#pragma unroll
    for (int z = 0; z < 16; ++z) {
      const int j = z >> 2, i = z & 3;
      if (z == 0) { LGKM(6); SBAR(); }
      if (z == 4) { if (more) LGKM(4); else LGKM(0); SBAR(); }
      const s16x8 bb = cat(b[c][j][0], b[c][j][1]);
      if (VT) mfma16i(acc[i][j], a[c][i], bb); else mfma16i(acc[i][j], bb, a[c][i]);
      SBAR();
      if (more && z < 12) rd(k + 1, z);
      if (z >= 12) { mfma4i(cs4[z & 1], ones, b[c][z & 1][(z >> 1) & 1]); SBAR(); }
    }
  }
  MFMA_DONE();
  STAMP(2);
  { const float c0 = sum32(sum16(cs4[0][0])), c1 = sum32(sum16(cs4[1][0]));
    if (g < 2) ((LAS float*)(smem + L_P1ST))[256 + wn * 64 + wm * 32 + g * 16 + r] = g ? c1 : c0; }
  WAITL_BAR();
  STAMP(20);
  const rsrc_t rw = mkrs(ws, WS_END);
  const LAS float* st = (const LAS float*)(smem + L_P1ST);
  const int tok = mb * 128 + wm * 64;
  auto swp = [](unsigned x0, unsigned x1, unsigned& lo, unsigned& hi) { auto t = __builtin_amdgcn_permlane16_swap(x0, x1, false, false); lo = t[0]; hi = t[1]; };
  if (!VT) {
    const bool isq = nb < 5;
    const float sc = isq ? QSCALE : 1.f;
    const unsigned base = isq ? WS_Q + (tok >> 6) * 2560 : WS_KV + (tok >> 8) * 20480 + ((tok >> 6) & 3) * 2560, hs = isq ? 81920 : 163840;
    float mu[4], rs[4]; f32x4 cs[4];
#pragma unroll
    for (int z = 0; z < 4; ++z) { mu[z] = st[row + z * 16]; rs[z] = st[128 + row + z * 16] * sc; cs[z] = *(const LAS f32x4*)(st + 256 + wn * 64 + (z ^ (2 * wm)) * 16 + 4 * g); }
    const int t32 = (g & 1) * 16 + r;
#pragma unroll
    for (int j = 0; j < 4; ++j) {
      const int c0 = (nb % 5) * 16 + wn * 8 + (j ^ (2 * wm)) * 2, c8 = c0 + (g >> 1), hp = (c8 * 52429) >> 18, pos = c8 - hp * 5;
      const unsigned hb = base + hp * 2 * hs;
      const unsigned om = pos == 2 ? 0x7fff0000u : hb + (pos > 2 ? hs + (pos - 3) * 512 : pos * 512) + t32 * 16, os = pos == 2 ? hb + 2048 + t32 * 8 : 0x7fff0000u;
      const bool any = c0 % 5 == 2 || c0 % 5 == 1;
#pragma unroll
      for (int i = 0; i < 4; i += 2) {
        u32x4 d;
#pragma unroll
        for (int e = 0; e < 2; ++e) {
          unsigned lo, hi;
          swp(pk((acc[i][j][2 * e] - mu[i] * cs[j][2 * e]) * rs[i], (acc[i][j][2 * e + 1] - mu[i] * cs[j][2 * e + 1]) * rs[i]),
              pk((acc[i + 1][j][2 * e] - mu[i + 1] * cs[j][2 * e]) * rs[i + 1], (acc[i + 1][j][2 * e + 1] - mu[i + 1] * cs[j][2 * e + 1]) * rs[i + 1]), lo, hi);
          d[e] = lo; d[2 + e] = hi;
        }
        __builtin_amdgcn_raw_buffer_store_b128(d, rw, om + i * 512, 0, SC1);
        if (any) { __builtin_amdgcn_raw_buffer_store_b64((u32x2){d[0], d[1]}, rw, os + i * 128, 0, SC1); __builtin_amdgcn_raw_buffer_store_b64((u32x2){d[2], d[3]}, rw, os + i * 128, hs, SC1); }
      }
    }
  } else {
    const unsigned base = WS_KV + (tok >> 8) * 20480 + 10240 + (((tok >> 3) & 31) + (g >> 1)) * 320;
    float cs[4]; f32x4 mu[4], rs[4];
#pragma unroll
    for (int z = 0; z < 4; ++z) { cs[z] = st[256 + wn * 64 + (z ^ (2 * wm)) * 16 + r]; mu[z] = *(const LAS f32x4*)(st + wm * 64 + z * 16 + 4 * g); rs[z] = *(const LAS f32x4*)(st + 128 + wm * 64 + z * 16 + 4 * g); }
#pragma unroll
    for (int j = 0; j < 4; j += 2) {
      const int f = (nb - 10) * 128 + wn * 64 + (j ^ (2 * wm)) * 16 + (g & 1) * 16 + r, head = (f * 52429) >> 20;
      const unsigned o = base + head * 163840 + (f - head * 20) * 16;
#pragma unroll
      for (int i = 0; i < 4; ++i) {
        u32x4 d;
#pragma unroll
        for (int e = 0; e < 2; ++e) {
          unsigned lo, hi;
          swp(pk((acc[i][j][2 * e] - mu[i][2 * e] * cs[j]) * rs[i][2 * e], (acc[i][j][2 * e + 1] - mu[i][2 * e + 1] * cs[j]) * rs[i][2 * e + 1]),
              pk((acc[i][j + 1][2 * e] - mu[i][2 * e] * cs[j + 1]) * rs[i][2 * e], (acc[i][j + 1][2 * e + 1] - mu[i][2 * e + 1] * cs[j + 1]) * rs[i][2 * e + 1]), lo, hi);
          d[e] = lo; d[2 + e] = hi;
        }
        __builtin_amdgcn_raw_buffer_store_b128(d, rw, o + i * 640, 0, SC1);
      }
    }
  }
  return tag;
}

#define LST(i)
#define DMA4(rs, lds, v, soff, A, B, POL) asm volatile("s_mov_b32 m0, %0\n\ts_nop 0\n\tbuffer_load_dwordx4 %1, %3, %4 offen offset:" #A POL " lds\n\tbuffer_load_dwordx4 %2, %3, %4 offen offset:" #B POL " lds" \
    :: "s"(lds), "v"((v)[(A) / 1024]), "v"((v)[(B) / 1024]), "s"(rs), "s"(soff) : "memory")
#define POLX ""
#define POLW ""
DI u64 p1_loader(const char* x, const char* wqkv, char* ws, LAS char* smem, int mb, int nb, int lw, int l STAMP_ARG) {
  const rsrc_t rx = mkrs(x, SEQ * DM * 2), rw = mkrs(wqkv, DM * 1920 * 2);
  const unsigned lds0 = (unsigned)(size_t)smem + lw * 4096;
  unsigned va[4], vw[4];
#pragma unroll
  for (int p = 0; p < 4; ++p) {
    va[p] = (mb * 128 + lw * 32 + p * 8 + (l >> 3)) * 1280 + ((((l & 7) ^ (l >> 4)) << 4) ^ ((p & 1) << 6)) - p * 1024;
    vw[p] = (lw * 16 + p * 4 + (l >> 4)) * 3840 + (((l & 15) ^ (((l >> 4) << 2) | (p & 2))) << 4) + nb * 256 - p * 1024;
  }
  int kt = STAG == 0 ? 0 : STAG == 1 ? (nb + 3 * mb) % 10 : STAG == 2 ? ((nb + mb) & 1) * 5 : (nb + mb) % 3;
  auto fill = [&](int slot) {
    const unsigned d = lds0 + slot * 32768, sa = kt * 128, sw = kt * (64 * 3840);
    DMA4(rx, d, va, sa, 0, 1024, POLX); DMA4(rw, d + 16384, vw, sw, 0, 1024, POLW); DMA4(rx, d, va, sa, 2048, 3072, POLX); DMA4(rw, d + 16384, vw, sw, 2048, 3072, POLW);
    kt = kt == 9 ? 0 : kt + 1;
  };
  STAMP(19);
  fill(0); fill(1);
  STAMP(20);
  WAITV_BAR(8); LST(8); fill(2);
  WAITV_BAR(8); LST(9); fill(3);
  WAITV_BAR(8); LST(10); fill(4);
#pragma unroll 1
  for (int slot = 0; slot < 5; ++slot) { WAITV_BAR(8); fill(slot); }
  WAITV_BAR(8); LST(16);
  const u64 epoch = __hip_atomic_load((GAS u64*)(ws + WS_EPOCH), __ATOMIC_RELAXED, __HIP_MEMORY_SCOPE_AGENT);
  GAS u64* xs = (GAS u64*)(ws + WS_XS + (mb * 128 + lw * 32) * 16 + l * 8);
  u64 g = __hip_atomic_load(xs, __ATOMIC_RELAXED, __HIP_MEMORY_SCOPE_AGENT);
  WAITV_BAR(0); LST(17);
  const u64 tag = mktag(epoch);
  for (int spin = 0; spin < (1 << 17) && !__all((unsigned)(g >> 32) == (unsigned)tag); ++spin) g = __hip_atomic_load(xs, __ATOMIC_RELAXED, __HIP_MEMORY_SCOPE_AGENT);
  STAMP(21);
  ((LAS unsigned*)(smem + L_P1ST))[(l & 1) * 128 + lw * 32 + (l >> 1)] = (unsigned)g;
  WAITL_BAR();
  return tag;
}

DI void p1_side(const char* w1, char* ws, LAS char* smem, int mb, int w, int l STAMP_ARG) {
  const rsrc_t r1 = mkrs(w1, mb < NCS ? DM * DFF * 2 : 0), rs = mkrs(ws, WS_END);
  const int t = threadIdx.x;
  u32x4 c[20];
#pragma unroll
  for (int z = 0; z < SIDE_WAIT; ++z) __builtin_amdgcn_s_sleep(16);
#pragma unroll
  for (int n = 0; n < 20; ++n) c[n] = __builtin_amdgcn_raw_buffer_load_b128(r1, (t >> 4) * 3456 + mb * 256 + (t & 15) * 16, n * 32 * 3456, 0);
  float s[8] = {};
#pragma unroll
  for (int n = 0; n < 20; ++n)
#pragma unroll
    for (int e = 0; e < 4; ++e) { s[2 * e] += bflo(c[n][e]); s[2 * e + 1] += bfhi(c[n][e]); }
#pragma unroll
  for (int e = 0; e < 8; ++e) s[e] = sum32(sum16(s[e]));
  if (l < 16) { *(LAS f32x4*)(smem + w * 512 + l * 32) = (f32x4){s[0], s[1], s[2], s[3]}; *(LAS f32x4*)(smem + w * 512 + l * 32 + 16) = (f32x4){s[4], s[5], s[6], s[7]}; }
  WAITL_BAR();
  if (t < 128 && mb < 14) { float a = 0.f;
#pragma unroll
    for (int n = 0; n < 8; ++n) a += ((LAS float*)smem)[n * 128 + t];
    __builtin_amdgcn_raw_buffer_store_b32(__float_as_uint(a), rs, WS_CS + (mb * 128 + t) * 4, 0, SC1); }
}

DI void rowmax_all(rsrc_t rs, unsigned kv, unsigned qp, int l, float& ma, float& mb_) {
  const s16x8 qa = ldfrag(rs, l * 16, qp), qb = ldfrag(rs, l * 16, qp + 1024);
  const s16x4 qr = __builtin_bit_cast(s16x4, __builtin_amdgcn_raw_buffer_load_b64(rs, l * 8, qp + 2048, SC1));
  float a = -INFINITY, b = -INFINITY;
#pragma unroll 1
  for (int t = 0; t < 64; ++t) {
    const unsigned kp = kv + (t >> 3) * 20480 + ((t >> 1) & 3) * 2560;
    const s16x4 kr = __builtin_bit_cast(s16x4, __builtin_amdgcn_raw_buffer_load_b64(rs, (l & 31) * 8, kp + 2048 + (t & 1) * 256, SC1));
    const s16x8 km = ldfrag(rs, l * 16, kp + (t & 1) * 1024);
    f32x32 s = {};
    s = __builtin_amdgcn_mfma_f32_32x32x4bf16_1k(kr, qr, s, 0, 0, 0);
    f32x16 sa = __builtin_shufflevector(s, s, 0, 1, 2, 3, 4, 5, 6, 7, 8, 9, 10, 11, 12, 13, 14, 15);
    f32x16 sb = __builtin_shufflevector(s, s, 16, 17, 18, 19, 20, 21, 22, 23, 24, 25, 26, 27, 28, 29, 30, 31);
    sa = mfma32(km, qa, sa); sb = mfma32(km, qb, sb);
#pragma unroll
    for (int e = 0; e < 16; ++e) { a = fmaxf(a, sa[e]); b = fmaxf(b, sb[e]); }
  }
  ma = max32(a); mb_ = max32(b);
}

DI int w1_slot(int n) { return n < 2 ? n : n + 5; }
DI unsigned w1_lane(int p, int l, int t4) { const int u = p * 64 + l, rr = (u * 4682) >> 16; return rr * 3232 + u * 16 + t4 * 32; }
DI void p2_attention(const char* wo, const char* w1, const char* w2, char* ws, LAS char* smem, int w0, int u1, u32x4 (&w1r)[15], u64 tag, u64 pc STAMP_ARG) {
  f32x16 oa, ob;
  int pass = 0;
  for (;;) {
    asm volatile("" : "+s"(pass));
    if (pass == 0 && w0 < 4) { WAITA_BAR(); STAMP(4); }
    int b = blockIdx.x, w = w0; asm volatile("" : "+s"(b), "+s"(w), "+s"(wo), "+s"(w1), "+s"(w2), "+s"(ws), "+s"(pc));
    const int xcd = b & 7, bi = b >> 3, head = xcd * 4 + (bi & 3), qblk = bi >> 2, qg = w & 3, kh = w >> 2;
    const int nb80 = bi & 7, t4 = ((bi >> 3) & 1) * 54 + 7 * nb80 - (nb80 >> 2);
    const rsrc_t rs = mkrs(ws, WS_END), ro = mkrs(wo, w < 5 ? DM * DM * 2 : 0), r1 = mkrs(w1, w < 7 ? DM * DFF * 2 : 0);
    const unsigned lds0 = (unsigned)(size_t)smem, kv0 = WS_KV + head * 163840, qp = WS_Q + head * 81920 + (qblk * 4 + qg) * 2560;
    int l = lane_again(), hh = l >> 5, r = l & 31;
    const unsigned vd = l * 16;
    const int uo = (w < 5 ? w : 0) * 64 + l, uor = (uo * 6554) >> 16;
    unsigned vwo = uor * 1280 + (uo - uor * 10) * 16 + nb80 * 160, vw1 = w1_lane(w < 7 ? w : 6, l, t4);
    const bool cst = r >= 20;
    unsigned ak = lds0 + kh * 5120 + l * 16, akr = lds0 + kh * 5120 + 2048 + l * 8;
    unsigned avc = lds0 + L_CONST + hh * 64 + 64 + (r > 20 ? 16 : 0) + (r >= 28 ? 128 : 0), av = lds0 + 10240 + kh * 5120 + hh * 320 + r * 16;
    asm volatile("" : "+v"(avc), "+v"(av));
    unsigned av1 = cst ? avc : av + 20480, av2 = cst ? avc : av + 40960; av = cst ? avc : av;
    unsigned pw = lds0 + L_PROG + w * 4, pr = lds0 + L_PROG + (l & 7) * 4;
    const int e0 = w * 64 + l, e1 = e0 + 512;
    const int q0 = (e0 * 3277) >> 16, q1 = (e1 * 3277) >> 16;
    unsigned t2a = q0 * 960 + e0 * 16, t2b = q1 * 960 + e1 * 16, t2r = lds0 + L_W2S + (4 * (l >> 4) + ((l & 15) >> 2)) * 320 + (l & 3) * 8 + w * 32;
    const rsrc_t r2 = mkrs(w2, b < 216 ? DFF * DM * 2 : 0);
    unsigned s2 = (b >> 2) * 40960 + (b & 3) * 320, l2 = lds0 + L_W2S + w * 1024;
    unsigned s2b = w < 2 ? s2 : 0x70000000u, l2b = w < 2 ? l2 + 8192 : lds0 + L_DUMMY;
    const unsigned d0 = b < 216 ? WS_W2 + ((b & 3) * 108 + uperm(b >> 2)) * 5120 + w * 1024 : 0x70000000u;
    unsigned d2a = d0 + (w >= 5 ? 53 * 5120 : 0), d2b = w < 2 ? d0 + 54 * 5120 + 3072 : 0x70000000u;
    unsigned f0 = WS_FLAG + (F_O + (qblk * 8 + qg * 2 + kh) * 32 + head) * FSTR, f1 = WS_FLAG + (F_H + ((xcd * 4 + (bi >> 3)) * 2 + (qg >> 1)) * 16 + nb80 * 2 + (w & 1)) * FSTR;
    unsigned f2 = WS_FLAG + (F_U + ((xcd * 2 + (bi >> 4)) * 4 + qg) * 16 + (bi & 15)) * FSTR;
    asm volatile("" : "+s"(f0), "+s"(f1), "+s"(f2));
    unsigned vfl = l == 1 ? f1 : f0; vfl = l == 2 ? f2 : vfl;
    const unsigned idx = 0xDDDDDDDDu;
    unsigned lw = lds0 + w * 1024, gw = kv0 + w * 1024, los = w < 5 ? 5120 : 0, g2 = WS_W2 + (bi * 8 + w) * 1024, fw = WS_FW2 + b * 8;
    asm volatile("" : "+v"(vwo), "+v"(vw1), "+v"(ak), "+v"(akr), "+v"(av), "+v"(av1), "+v"(av2), "+v"(pw), "+v"(pr), "+v"(t2a), "+v"(t2b), "+v"(t2r), "+v"(vfl));
    unsigned lo = w < 5 ? lds0 + L_WO + w * 1024 : lds0 + L_DUMMY, qa = qp + kh * 1024, qb = qp + 1024 - kh * 1024, qx = kh * 256, co = w == 0 ? 12288 + bi * 1024 : 0x70000000;
    rsrc_t ro_ = ro, r1_ = r1, r2_ = r2, rc_ = mkrs((const void*)(pc & ~255ull), CODE_PF);
    asm volatile("" : "+s"(lo), "+s"(qa), "+s"(qb), "+s"(qx), "+s"(co), "+s"(lw), "+s"(gw), "+s"(los), "+s"(g2), "+s"(fw), "+s"(s2), "+s"(l2), "+s"(s2b), "+s"(l2b), "+s"(d2a), "+s"(d2b),
                 "+s"(ro_), "+s"(r1_), "+s"(r2_), "+s"(rc_));
    float ma = 0.f, mb_ = 0.f;
    if (pass == 0) {
      if (w >= 4) WAITA_BAR();
      if (w == 4) {
        if (l == 0) __hip_atomic_store((GAS u64*)(ws + WS_FLAG + u1 * QSTR), tag, __ATOMIC_RELAXED, __HIP_MEMORY_SCOPE_AGENT);
        const int n0 = (head * 20) >> 7, n1 = (head * 20 + 19) >> 7;
        const int fi[2] = {((l & 32 ? 10 : 5) + (l & 16 ? n1 : n0)) * 16 + (l & 15), (l & 2 ? n1 : n0) * 16 + qblk * 2 + (l & 1)};
        if (PD1) __builtin_amdgcn_s_sleep(PD1);
        await<2, QSTR, WS_FLAG, QSL>(ws, fi, tag);
      } else if (w > 4) {
        const int t = (w - 5) * 64 + l;
#pragma unroll
        for (int z = 0; z < 2; ++z) if (t + z * 192 < 304) { const unsigned o2 = (t & 3) == 0 ? 0x3F803F80u : 0u; *(LAS u32x4*)(smem + L_CONST + (t + z * 192) * 16) = (u32x4){o2, o2, o2, o2}; }
      }
    } else { rowmax_all(rs, kv0, qp, l, ma, mb_); if (kh) { const float t = ma; ma = mb_; mb_ = t; } }
    if (w == 7 && l < 8) *(LAS unsigned*)(smem + L_PROG + l * 4) = 0;
    BAR();
    STAMP(5);
    unsigned t0; u32x4 wo2a, wo2b;
    asm volatile(
      "s_nop 3\n\t"
      "v_lshrrev_b32 v225, 1, v219\n\t"
      "v_xor_b32 v225, %[qx], v225\n\t"
      "buffer_load_dwordx4 v[192:195], v219, %[rs], %[qa] offen sc1\n\t"
      "buffer_load_dwordx4 v[196:199], v219, %[rs], %[qb] offen sc1\n\t"
      "buffer_load_dwordx2 v[200:201], v225, %[rs], %[qp] offen offset:2048 sc1\n\t"
      "s_cmp_lt_u32 %[wv], 4\n\t"
      "s_cbranch_scc1 21f\n\t"
      "s_add_u32 m0, %[lw], 12288\n\t"
      "s_add_u32 %[t0], %[gw], 12288\n\t"
      "buffer_load_dwordx4 v219, %[rs], %[t0] offen sc1 lds\n\t"
      "21:\n\t"
      "s_add_u32 m0, %[lw], 0\n\t"
      "s_add_u32 %[t0], %[gw], 0\n\t"
      "buffer_load_dwordx4 v219, %[rs], %[t0] offen sc1 lds\n\t"
      "s_add_u32 m0, %[lw], 8192\n\t"
      "s_add_u32 %[t0], %[gw], 8192\n\t"
      "buffer_load_dwordx4 v219, %[rs], %[t0] offen sc1 lds\n\t"
      "s_cmp_lt_u32 %[wv], 4\n\t"
      "s_cbranch_scc1 22f\n\t"
      "s_add_u32 m0, %[lw], 32768\n\t"
      "s_add_u32 %[t0], %[gw], 32768\n\t"
      "buffer_load_dwordx4 v219, %[rs], %[t0] offen sc1 lds\n\t"
      "22:\n\t"
      "s_add_u32 m0, %[lw], 20480\n\t"
      "s_add_u32 %[t0], %[gw], 20480\n\t"
      "buffer_load_dwordx4 v219, %[rs], %[t0] offen sc1 lds\n\t"
      "s_add_u32 m0, %[lw], 28672\n\t"
      "s_add_u32 %[t0], %[gw], 28672\n\t"
      "buffer_load_dwordx4 v219, %[rs], %[t0] offen sc1 lds\n\t"
      "v_mov_b64 v[128:129], 0\n\t"
      "v_mov_b64 v[130:131], 0\n\t"
      "v_mov_b64 v[132:133], 0\n\t"
      "v_mov_b64 v[134:135], 0\n\t"
      "v_mov_b64 v[136:137], 0\n\t"
      "v_mov_b64 v[138:139], 0\n\t"
      "v_mov_b64 v[140:141], 0\n\t"
      "v_mov_b64 v[142:143], 0\n\t"
      "v_mov_b64 v[144:145], 0\n\t"
      "v_mov_b64 v[146:147], 0\n\t"
      "v_mov_b64 v[148:149], 0\n\t"
      "v_mov_b64 v[150:151], 0\n\t"
      "v_mov_b64 v[152:153], 0\n\t"
      "v_mov_b64 v[154:155], 0\n\t"
      "v_mov_b64 v[156:157], 0\n\t"
      "v_mov_b64 v[158:159], 0\n\t"
      "s_waitcnt vmcnt(2) lgkmcnt(0)\n\t"
      "s_barrier\n\t"
      "ds_read_b128 v[204:207], v217 offset:0\n\t"
      "ds_read_b64 v[202:203], v218 offset:0\n\t"
      "ds_read_b128 v[208:211], v220 offset:0\n\t"
      "s_waitcnt lgkmcnt(1)\n\t"
      "v_mfma_f32_32x32x16_bf16 v[64:79], v[204:207], v[192:195], 0\n\t"
      "v_mfma_f32_32x32x16_bf16 v[80:95], v[204:207], v[196:199], 0\n\t"
      "ds_read_b128 v[204:207], v217 offset:1024\n\t"
      "s_nop 8\n\t"
      "v_mfma_f32_32x32x4_2b_bf16 v[64:95], v[202:203], v[200:201], v[64:95] cbsz:1 abid:0\n\t"
      "s_cmp_lg_u32 %[pass], 0\n\t"
      "s_nop 15\n\t"
      "s_nop 2\n\t"
      "s_cbranch_scc1 23f\n\t"
      "v_max3_f32 v242, v64, v65, v66\n\t"
      "v_max3_f32 v243, v80, v81, v82\n\t"
      "v_max3_f32 v242, v242, v67, v68\n\t"
      "v_max3_f32 v243, v243, v83, v84\n\t"
      "v_max3_f32 v242, v242, v69, v70\n\t"
      "v_max3_f32 v243, v243, v85, v86\n\t"
      "v_max3_f32 v242, v242, v71, v72\n\t"
      "v_max3_f32 v243, v243, v87, v88\n\t"
      "v_max3_f32 v242, v242, v73, v74\n\t"
      "v_max3_f32 v243, v243, v89, v90\n\t"
      "v_max3_f32 v242, v242, v75, v76\n\t"
      "v_max3_f32 v243, v243, v91, v92\n\t"
      "v_max3_f32 v242, v242, v77, v78\n\t"
      "v_max3_f32 v243, v243, v93, v94\n\t"
      "v_max_f32 v242, v242, v79\n\t"
      "v_max_f32 v243, v243, v95\n\t"
      "v_mov_b32 v225, v242\n\t"
      "v_mov_b32 v226, v243\n\t"
      "s_nop 1\n\t"
      "v_permlane32_swap_b32 v242, v225\n\t"
      "v_permlane32_swap_b32 v243, v226\n\t"
      "v_max_f32 v242, v242, v225\n\t"
      "v_max_f32 v243, v243, v226\n\t"
      "23:\n\t"
      "v_sub_f32 v160, 0, v242\n\t"
      "v_sub_f32 v176, 0, v243\n\t"
      "v_sub_f32 v161, 0, v242\n\t"
      "v_sub_f32 v177, 0, v243\n\t"
      "v_mov_b64 v[162:163], v[160:161]\n\t"
      "v_mov_b64 v[178:179], v[176:177]\n\t"
      "v_mov_b64 v[164:165], v[160:161]\n\t"
      "v_mov_b64 v[180:181], v[176:177]\n\t"
      "v_mov_b64 v[166:167], v[160:161]\n\t"
      "v_mov_b64 v[182:183], v[176:177]\n\t"
      "v_mov_b64 v[168:169], v[160:161]\n\t"
      "v_mov_b64 v[184:185], v[176:177]\n\t"
      "v_mov_b64 v[170:171], v[160:161]\n\t"
      "v_mov_b64 v[186:187], v[176:177]\n\t"
      "v_mov_b64 v[172:173], v[160:161]\n\t"
      "v_mov_b64 v[188:189], v[176:177]\n\t"
      "v_mov_b64 v[174:175], v[160:161]\n\t"
      "v_mov_b64 v[190:191], v[176:177]\n\t"
      "v_pk_add_f32 v[64:65], v[64:65], v[160:161]\n\t"
      "v_pk_add_f32 v[80:81], v[80:81], v[176:177]\n\t"
      "v_pk_add_f32 v[66:67], v[66:67], v[160:161]\n\t"
      "v_pk_add_f32 v[82:83], v[82:83], v[176:177]\n\t"
      "v_pk_add_f32 v[68:69], v[68:69], v[160:161]\n\t"
      "v_pk_add_f32 v[84:85], v[84:85], v[176:177]\n\t"
      "v_pk_add_f32 v[70:71], v[70:71], v[160:161]\n\t"
      "v_pk_add_f32 v[86:87], v[86:87], v[176:177]\n\t"
      "v_pk_add_f32 v[72:73], v[72:73], v[160:161]\n\t"
      "v_pk_add_f32 v[88:89], v[88:89], v[176:177]\n\t"
      "v_pk_add_f32 v[74:75], v[74:75], v[160:161]\n\t"
      "v_pk_add_f32 v[90:91], v[90:91], v[176:177]\n\t"
      "v_pk_add_f32 v[76:77], v[76:77], v[160:161]\n\t"
      "v_pk_add_f32 v[92:93], v[92:93], v[176:177]\n\t"
      "v_pk_add_f32 v[78:79], v[78:79], v[160:161]\n\t"
      "v_pk_add_f32 v[94:95], v[94:95], v[176:177]\n\t"
      "s_waitcnt lgkmcnt(0)\n\t"
      "s_setprio 3\n\t"
      "s_waitcnt lgkmcnt(1)\n\t"
      "v_mfma_f32_32x32x16_bf16 v[96:111], v[204:207], v[192:195], v[160:175]\n\t"
      "v_exp_f32 v64, v64\n\t"
      "s_waitcnt vmcnt(0)\n\t"
      "v_mov_b32 v225, 1\n\t"
      "v_exp_f32 v65, v65\n\t"
      "ds_write_b32 v223, v225\n\t"
      "v_exp_f32 v66, v66\n\t"
      "v_exp_f32 v67, v67\n\t"
      "ds_read_b128 v[212:215], v220 offset:640\n\t"
      "v_exp_f32 v68, v68\n\t"
      "v_exp_f32 v69, v69\n\t"
      "v_exp_f32 v70, v70\n\t"
      "v_exp_f32 v71, v71\n\t"
      "v_mfma_f32_32x32x16_bf16 v[112:127], v[204:207], v[196:199], v[176:191]\n\t"
      "v_exp_f32 v72, v72\n\t"
      "v_exp_f32 v73, v73\n\t"
      "v_exp_f32 v74, v74\n\t"
      "v_exp_f32 v75, v75\n\t"
      "s_waitcnt lgkmcnt(1)\n\t"
      "v_smfmac_f32_32x32x32_bf16 v[128:143], v[208:211], v[64:71], v216\n\t"
      "ds_read_b128 v[204:207], v217 offset:2560\n\t"
      "v_exp_f32 v76, v76\n\t"
      "v_exp_f32 v77, v77\n\t"
      "v_exp_f32 v78, v78\n\t"
      "v_exp_f32 v79, v79\n\t"
      "v_mfma_f32_32x32x4_2b_bf16 v[96:127], v[202:203], v[200:201], v[96:127] cbsz:1 abid:1\n\t"
      "ds_read_b64 v[202:203], v218 offset:2560\n\t"
      "v_exp_f32 v80, v80\n\t"
      "v_exp_f32 v81, v81\n\t"
      "v_exp_f32 v82, v82\n\t"
      "v_exp_f32 v83, v83\n\t"
      "v_exp_f32 v84, v84\n\t"
      "v_exp_f32 v85, v85\n\t"
      "v_exp_f32 v86, v86\n\t"
      "v_exp_f32 v87, v87\n\t"
      "s_waitcnt lgkmcnt(2)\n\t"
      "v_smfmac_f32_32x32x32_bf16 v[128:143], v[212:215], v[72:79], v216\n\t"
      "v_exp_f32 v88, v88\n\t"
      "v_exp_f32 v89, v89\n\t"
      "v_exp_f32 v90, v90\n\t"
      "v_exp_f32 v91, v91\n\t"
      "v_smfmac_f32_32x32x32_bf16 v[144:159], v[208:211], v[80:87], v216\n\t"
      "ds_read_b128 v[208:211], v220 offset:1280\n\t"
      "v_exp_f32 v92, v92\n\t"
      "v_exp_f32 v93, v93\n\t"
      "v_exp_f32 v94, v94\n\t"
      "v_exp_f32 v95, v95\n\t"
      "s_setprio 2\n\t"
      "s_waitcnt lgkmcnt(1)\n\t"
      "v_mfma_f32_32x32x16_bf16 v[64:79], v[204:207], v[192:195], v[160:175]\n\t"
      "v_exp_f32 v96, v96\n\t"
      "v_exp_f32 v97, v97\n\t"
      "v_exp_f32 v98, v98\n\t"
      "v_exp_f32 v99, v99\n\t"
      "v_smfmac_f32_32x32x32_bf16 v[144:159], v[212:215], v[88:95], v216\n\t"
      "ds_read_b128 v[212:215], v220 offset:1920\n\t"
      "v_exp_f32 v100, v100\n\t"
      "v_exp_f32 v101, v101\n\t"
      "v_exp_f32 v102, v102\n\t"
      "v_exp_f32 v103, v103\n\t"
      "v_mfma_f32_32x32x16_bf16 v[80:95], v[204:207], v[196:199], v[176:191]\n\t"
      "v_exp_f32 v104, v104\n\t"
      "v_exp_f32 v105, v105\n\t"
      "v_exp_f32 v106, v106\n\t"
      "v_exp_f32 v107, v107\n\t"
      "s_waitcnt lgkmcnt(1)\n\t"
      "v_smfmac_f32_32x32x32_bf16 v[128:143], v[208:211], v[96:103], v216\n\t"
      "ds_read_b128 v[204:207], v217 offset:3584\n\t"
      "v_exp_f32 v108, v108\n\t"
      "v_exp_f32 v109, v109\n\t"
      "v_exp_f32 v110, v110\n\t"
      "v_exp_f32 v111, v111\n\t"
      "v_mfma_f32_32x32x4_2b_bf16 v[64:95], v[202:203], v[200:201], v[64:95] cbsz:1 abid:0\n\t"
      "v_exp_f32 v112, v112\n\t"
      "v_exp_f32 v113, v113\n\t"
      "v_exp_f32 v114, v114\n\t"
      "v_exp_f32 v115, v115\n\t"
      "v_exp_f32 v116, v116\n\t"
      "v_exp_f32 v117, v117\n\t"
      "v_exp_f32 v118, v118\n\t"
      "v_exp_f32 v119, v119\n\t"
      "s_waitcnt lgkmcnt(1)\n\t"
      "v_smfmac_f32_32x32x32_bf16 v[128:143], v[212:215], v[104:111], v216\n\t"
      "ds_read_b32 v226, v224\n\t"
      "v_exp_f32 v120, v120\n\t"
      "v_exp_f32 v121, v121\n\t"
      "v_exp_f32 v122, v122\n\t"
      "v_exp_f32 v123, v123\n\t"
      "v_smfmac_f32_32x32x32_bf16 v[144:159], v[208:211], v[112:119], v216\n\t"
      "ds_read_b128 v[208:211], v220 offset:2560\n\t"
      "v_exp_f32 v124, v124\n\t"
      "v_exp_f32 v125, v125\n\t"
      "v_exp_f32 v126, v126\n\t"
      "v_exp_f32 v127, v127\n\t"
      "s_setprio 1\n\t"
      "s_waitcnt lgkmcnt(1)\n\t"
      "v_cmp_gt_u32 vcc, 1, v226\n\t"
      "s_cbranch_vccz 25f\n\t"
      "24:\n\t"
      "ds_read_b32 v226, v224\n\t"
      "s_waitcnt lgkmcnt(0)\n\t"
      "v_cmp_gt_u32 vcc, 1, v226\n\t"
      "s_cbranch_vccnz 24b\n\t"
      "25:\n\t"
      "v_mfma_f32_32x32x16_bf16 v[96:111], v[204:207], v[192:195], v[160:175]\n\t"
      "v_exp_f32 v64, v64\n\t"
      "v_exp_f32 v65, v65\n\t"
      "s_cmp_lt_u32 %[wv], 4\n\t"
      "s_cbranch_scc1 26f\n\t"
      "s_add_u32 m0, %[lw], 53248\n\t"
      "s_add_u32 %[t0], %[gw], 53248\n\t"
      "buffer_load_dwordx4 v219, %[rs], %[t0] offen sc1 lds\n\t"
      "26:\n\t"
      "s_add_u32 m0, %[lw], 40960\n\t"
      "s_add_u32 %[t0], %[gw], 40960\n\t"
      "buffer_load_dwordx4 v219, %[rs], %[t0] offen sc1 lds\n\t"
      "v_exp_f32 v66, v66\n\t"
      "v_exp_f32 v67, v67\n\t"
      "v_smfmac_f32_32x32x32_bf16 v[144:159], v[212:215], v[120:127], v216\n\t"
      "ds_read_b128 v[212:215], v220 offset:3200\n\t"
      "v_exp_f32 v68, v68\n\t"
      "v_exp_f32 v69, v69\n\t"
      "s_add_u32 m0, %[lw], 49152\n\t"
      "s_add_u32 %[t0], %[gw], 49152\n\t"
      "buffer_load_dwordx4 v219, %[rs], %[t0] offen sc1 lds\n\t"
      "v_exp_f32 v70, v70\n\t"
      "v_exp_f32 v71, v71\n\t"
      "v_mfma_f32_32x32x16_bf16 v[112:127], v[204:207], v[196:199], v[176:191]\n\t"
      "v_exp_f32 v72, v72\n\t"
      "v_exp_f32 v73, v73\n\t"
      "v_exp_f32 v74, v74\n\t"
      "v_exp_f32 v75, v75\n\t"
      "s_mov_b32 m0, %[l2]\n\t"
      "s_nop 0\n\t"
      "buffer_load_dwordx4 v234, %[r2], %[s2] offen lds\n\t"
      "s_waitcnt lgkmcnt(1)\n\t"
      "v_smfmac_f32_32x32x32_bf16 v[128:143], v[208:211], v[64:71], v216\n\t"
      "ds_read_b128 v[204:207], v217 offset:20480\n\t"
      "v_exp_f32 v76, v76\n\t"
      "v_exp_f32 v77, v77\n\t"
      "v_exp_f32 v78, v78\n\t"
      "v_exp_f32 v79, v79\n\t"
      "v_mfma_f32_32x32x4_2b_bf16 v[96:127], v[202:203], v[200:201], v[96:127] cbsz:1 abid:1\n\t"
      "ds_read_b64 v[202:203], v218 offset:20480\n\t"
      "v_exp_f32 v80, v80\n\t"
      "v_exp_f32 v81, v81\n\t"
      "v_exp_f32 v82, v82\n\t"
      "v_exp_f32 v83, v83\n\t"
      "v_exp_f32 v84, v84\n\t"
      "v_exp_f32 v85, v85\n\t"
      "v_exp_f32 v86, v86\n\t"
      "v_exp_f32 v87, v87\n\t"
      "s_mov_b32 m0, %[l2b]\n\t"
      "s_nop 0\n\t"
      "buffer_load_dwordx4 v235, %[r2], %[s2b] offen lds\n\t"
      "s_waitcnt lgkmcnt(2)\n\t"
      "v_smfmac_f32_32x32x32_bf16 v[128:143], v[212:215], v[72:79], v216\n\t"
      "v_exp_f32 v88, v88\n\t"
      "v_exp_f32 v89, v89\n\t"
      "v_exp_f32 v90, v90\n\t"
      "v_exp_f32 v91, v91\n\t"
      "v_smfmac_f32_32x32x32_bf16 v[144:159], v[208:211], v[80:87], v216\n\t"
      "ds_read_b128 v[208:211], v220 offset:3840\n\t"
      "v_exp_f32 v92, v92\n\t"
      "v_exp_f32 v93, v93\n\t"
      "v_exp_f32 v94, v94\n\t"
      "v_exp_f32 v95, v95\n\t"
      "s_setprio 0\n\t"
      "s_waitcnt lgkmcnt(1)\n\t"
      "v_mfma_f32_32x32x16_bf16 v[64:79], v[204:207], v[192:195], v[160:175]\n\t"
      "v_exp_f32 v96, v96\n\t"
      "v_exp_f32 v97, v97\n\t"
      "v_exp_f32 v98, v98\n\t"
      "v_exp_f32 v99, v99\n\t"
      "v_smfmac_f32_32x32x32_bf16 v[144:159], v[212:215], v[88:95], v216\n\t"
      "ds_read_b128 v[212:215], v220 offset:4480\n\t"
      "v_exp_f32 v100, v100\n\t"
      "v_exp_f32 v101, v101\n\t"
      "v_exp_f32 v102, v102\n\t"
      "v_exp_f32 v103, v103\n\t"
      "v_mfma_f32_32x32x16_bf16 v[80:95], v[204:207], v[196:199], v[176:191]\n\t"
      "v_exp_f32 v104, v104\n\t"
      "v_exp_f32 v105, v105\n\t"
      "v_exp_f32 v106, v106\n\t"
      "v_exp_f32 v107, v107\n\t"
      "s_waitcnt lgkmcnt(1)\n\t"
      "v_smfmac_f32_32x32x32_bf16 v[128:143], v[208:211], v[96:103], v216\n\t"
      "ds_read_b128 v[204:207], v217 offset:21504\n\t"
      "v_exp_f32 v108, v108\n\t"
      "v_exp_f32 v109, v109\n\t"
      "v_exp_f32 v110, v110\n\t"
      "v_exp_f32 v111, v111\n\t"
      "v_mfma_f32_32x32x4_2b_bf16 v[64:95], v[202:203], v[200:201], v[64:95] cbsz:1 abid:0\n\t"
      "v_exp_f32 v112, v112\n\t"
      "v_exp_f32 v113, v113\n\t"
      "v_exp_f32 v114, v114\n\t"
      "v_exp_f32 v115, v115\n\t"
      "v_exp_f32 v116, v116\n\t"
      "v_exp_f32 v117, v117\n\t"
      "v_exp_f32 v118, v118\n\t"
      "v_exp_f32 v119, v119\n\t"
      "s_waitcnt lgkmcnt(1)\n\t"
      "v_smfmac_f32_32x32x32_bf16 v[128:143], v[212:215], v[104:111], v216\n\t"
      "v_exp_f32 v120, v120\n\t"
      "v_exp_f32 v121, v121\n\t"
      "v_exp_f32 v122, v122\n\t"
      "v_exp_f32 v123, v123\n\t"
      "v_smfmac_f32_32x32x32_bf16 v[144:159], v[208:211], v[112:119], v216\n\t"
      "ds_read_b128 v[208:211], v221 offset:0\n\t"
      "v_exp_f32 v124, v124\n\t"
      "v_exp_f32 v125, v125\n\t"
      "v_exp_f32 v126, v126\n\t"
      "v_exp_f32 v127, v127\n\t"
      "s_setprio 3\n\t"
      "s_waitcnt lgkmcnt(1)\n\t"
      "v_mfma_f32_32x32x16_bf16 v[96:111], v[204:207], v[192:195], v[160:175]\n\t"
      "v_exp_f32 v64, v64\n\t"
      "s_waitcnt vmcnt(2)\n\t"
      "v_mov_b32 v225, 2\n\t"
      "v_exp_f32 v65, v65\n\t"
      "ds_write_b32 v223, v225\n\t"
      "v_exp_f32 v66, v66\n\t"
      "v_exp_f32 v67, v67\n\t"
      "v_smfmac_f32_32x32x32_bf16 v[144:159], v[212:215], v[120:127], v216\n\t"
      "ds_read_b128 v[212:215], v221 offset:640\n\t"
      "v_exp_f32 v68, v68\n\t"
      "v_exp_f32 v69, v69\n\t"
      "v_exp_f32 v70, v70\n\t"
      "v_exp_f32 v71, v71\n\t"
      "buffer_load_dword v228, v233, %[rs], 0 offen sc1\n\t"
      "v_mfma_f32_32x32x16_bf16 v[112:127], v[204:207], v[196:199], v[176:191]\n\t"
      "v_exp_f32 v72, v72\n\t"
      "v_exp_f32 v73, v73\n\t"
      "v_exp_f32 v74, v74\n\t"
      "v_exp_f32 v75, v75\n\t"
      "s_waitcnt lgkmcnt(1)\n\t"
      "v_smfmac_f32_32x32x32_bf16 v[128:143], v[208:211], v[64:71], v216\n\t"
      "ds_read_b128 v[204:207], v217 offset:23040\n\t"
      "v_exp_f32 v76, v76\n\t"
      "v_exp_f32 v77, v77\n\t"
      "v_exp_f32 v78, v78\n\t"
      "v_exp_f32 v79, v79\n\t"
      "v_mfma_f32_32x32x4_2b_bf16 v[96:127], v[202:203], v[200:201], v[96:127] cbsz:1 abid:1\n\t"
      "ds_read_b64 v[202:203], v218 offset:23040\n\t"
      "v_exp_f32 v80, v80\n\t"
      "v_exp_f32 v81, v81\n\t"
      "v_exp_f32 v82, v82\n\t"
      "v_exp_f32 v83, v83\n\t"
      "v_exp_f32 v84, v84\n\t"
      "v_exp_f32 v85, v85\n\t"
      "v_exp_f32 v86, v86\n\t"
      "v_exp_f32 v87, v87\n\t"
      "s_waitcnt lgkmcnt(2)\n\t"
      "v_smfmac_f32_32x32x32_bf16 v[128:143], v[212:215], v[72:79], v216\n\t"
      "v_exp_f32 v88, v88\n\t"
      "v_exp_f32 v89, v89\n\t"
      "v_exp_f32 v90, v90\n\t"
      "v_exp_f32 v91, v91\n\t"
      "v_smfmac_f32_32x32x32_bf16 v[144:159], v[208:211], v[80:87], v216\n\t"
      "ds_read_b128 v[208:211], v221 offset:1280\n\t"
      "v_exp_f32 v92, v92\n\t"
      "v_exp_f32 v93, v93\n\t"
      "v_exp_f32 v94, v94\n\t"
      "v_exp_f32 v95, v95\n\t"
      "s_setprio 2\n\t"
      "s_waitcnt lgkmcnt(1)\n\t"
      "v_mfma_f32_32x32x16_bf16 v[64:79], v[204:207], v[192:195], v[160:175]\n\t"
      "v_exp_f32 v96, v96\n\t"
      "v_exp_f32 v97, v97\n\t"
      "v_exp_f32 v98, v98\n\t"
      "v_exp_f32 v99, v99\n\t"
      "v_smfmac_f32_32x32x32_bf16 v[144:159], v[212:215], v[88:95], v216\n\t"
      "ds_read_b128 v[212:215], v221 offset:1920\n\t"
      "v_exp_f32 v100, v100\n\t"
      "v_exp_f32 v101, v101\n\t"
      "v_exp_f32 v102, v102\n\t"
      "v_exp_f32 v103, v103\n\t"
      "v_mfma_f32_32x32x16_bf16 v[80:95], v[204:207], v[196:199], v[176:191]\n\t"
      "v_exp_f32 v104, v104\n\t"
      "v_exp_f32 v105, v105\n\t"
      "v_exp_f32 v106, v106\n\t"
      "v_exp_f32 v107, v107\n\t"
      "s_waitcnt lgkmcnt(1)\n\t"
      "v_smfmac_f32_32x32x32_bf16 v[128:143], v[208:211], v[96:103], v216\n\t"
      "ds_read_b128 v[204:207], v217 offset:24064\n\t"
      "v_exp_f32 v108, v108\n\t"
      "v_exp_f32 v109, v109\n\t"
      "v_exp_f32 v110, v110\n\t"
      "v_exp_f32 v111, v111\n\t"
      "v_mfma_f32_32x32x4_2b_bf16 v[64:95], v[202:203], v[200:201], v[64:95] cbsz:1 abid:0\n\t"
      "v_exp_f32 v112, v112\n\t"
      "v_exp_f32 v113, v113\n\t"
      "v_exp_f32 v114, v114\n\t"
      "v_exp_f32 v115, v115\n\t"
      "v_exp_f32 v116, v116\n\t"
      "v_exp_f32 v117, v117\n\t"
      "v_exp_f32 v118, v118\n\t"
      "v_exp_f32 v119, v119\n\t"
      "s_waitcnt lgkmcnt(1)\n\t"
      "v_smfmac_f32_32x32x32_bf16 v[128:143], v[212:215], v[104:111], v216\n\t"
      "ds_read_b32 v226, v224\n\t"
      "v_exp_f32 v120, v120\n\t"
      "v_exp_f32 v121, v121\n\t"
      "v_exp_f32 v122, v122\n\t"
      "v_exp_f32 v123, v123\n\t"
      "v_smfmac_f32_32x32x32_bf16 v[144:159], v[208:211], v[112:119], v216\n\t"
      "ds_read_b128 v[208:211], v221 offset:2560\n\t"
      "v_exp_f32 v124, v124\n\t"
      "v_exp_f32 v125, v125\n\t"
      "v_exp_f32 v126, v126\n\t"
      "v_exp_f32 v127, v127\n\t"
      "s_setprio 1\n\t"
      "s_waitcnt lgkmcnt(1)\n\t"
      "v_cmp_gt_u32 vcc, 2, v226\n\t"
      "s_cbranch_vccz 28f\n\t"
      "27:\n\t"
      "ds_read_b32 v226, v224\n\t"
      "s_waitcnt lgkmcnt(0)\n\t"
      "v_cmp_gt_u32 vcc, 2, v226\n\t"
      "s_cbranch_vccnz 27b\n\t"
      "28:\n\t"
      "v_mfma_f32_32x32x16_bf16 v[96:111], v[204:207], v[192:195], v[160:175]\n\t"
      "v_exp_f32 v64, v64\n\t"
      "v_exp_f32 v65, v65\n\t"
      "s_cmp_lt_u32 %[wv], 4\n\t"
      "s_cbranch_scc1 29f\n\t"
      "s_add_u32 m0, %[lw], 12288\n\t"
      "s_add_u32 %[t0], %[gw], 73728\n\t"
      "buffer_load_dwordx4 v219, %[rs], %[t0] offen sc1 lds\n\t"
      "29:\n\t"
      "s_add_u32 m0, %[lw], 0\n\t"
      "s_add_u32 %[t0], %[gw], 61440\n\t"
      "buffer_load_dwordx4 v219, %[rs], %[t0] offen sc1 lds\n\t"
      "v_exp_f32 v66, v66\n\t"
      "v_exp_f32 v67, v67\n\t"
      "v_smfmac_f32_32x32x32_bf16 v[144:159], v[212:215], v[120:127], v216\n\t"
      "ds_read_b128 v[212:215], v221 offset:3200\n\t"
      "v_exp_f32 v68, v68\n\t"
      "v_exp_f32 v69, v69\n\t"
      "s_add_u32 m0, %[lw], 8192\n\t"
      "s_add_u32 %[t0], %[gw], 69632\n\t"
      "buffer_load_dwordx4 v219, %[rs], %[t0] offen sc1 lds\n\t"
      "v_exp_f32 v70, v70\n\t"
      "v_exp_f32 v71, v71\n\t"
      "s_mov_b32 %[t0], 0\n\t"
      "buffer_load_dwordx4 v[4:7], v232, %[r1], %[t0] offen\n\t"
      "v_mfma_f32_32x32x16_bf16 v[112:127], v[204:207], v[196:199], v[176:191]\n\t"
      "v_exp_f32 v72, v72\n\t"
      "v_exp_f32 v73, v73\n\t"
      "v_exp_f32 v74, v74\n\t"
      "v_exp_f32 v75, v75\n\t"
      "s_waitcnt lgkmcnt(1)\n\t"
      "v_smfmac_f32_32x32x32_bf16 v[128:143], v[208:211], v[64:71], v216\n\t"
      "ds_read_b128 v[204:207], v217 offset:40960\n\t"
      "v_exp_f32 v76, v76\n\t"
      "v_exp_f32 v77, v77\n\t"
      "v_exp_f32 v78, v78\n\t"
      "v_exp_f32 v79, v79\n\t"
      "v_mfma_f32_32x32x4_2b_bf16 v[96:127], v[202:203], v[200:201], v[96:127] cbsz:1 abid:1\n\t"
      "ds_read_b64 v[202:203], v218 offset:40960\n\t"
      "v_exp_f32 v80, v80\n\t"
      "v_exp_f32 v81, v81\n\t"
      "v_exp_f32 v82, v82\n\t"
      "v_exp_f32 v83, v83\n\t"
      "s_mov_b32 m0, %[lo]\n\t"
      "s_add_u32 %[lo], %[lo], %[los]\n\t"
      "s_mov_b32 %[t0], 0\n\t"
      "buffer_load_dwordx4 v227, %[ro], %[t0] offen lds\n\t"
      "v_exp_f32 v84, v84\n\t"
      "v_exp_f32 v85, v85\n\t"
      "v_exp_f32 v86, v86\n\t"
      "v_exp_f32 v87, v87\n\t"
      "s_waitcnt lgkmcnt(2)\n\t"
      "v_smfmac_f32_32x32x32_bf16 v[128:143], v[212:215], v[72:79], v216\n\t"
      "v_exp_f32 v88, v88\n\t"
      "v_exp_f32 v89, v89\n\t"
      "v_exp_f32 v90, v90\n\t"
      "v_exp_f32 v91, v91\n\t"
      "v_smfmac_f32_32x32x32_bf16 v[144:159], v[208:211], v[80:87], v216\n\t"
      "ds_read_b128 v[208:211], v221 offset:3840\n\t"
      "v_exp_f32 v92, v92\n\t"
      "v_exp_f32 v93, v93\n\t"
      "v_exp_f32 v94, v94\n\t"
      "v_exp_f32 v95, v95\n\t"
      "s_setprio 0\n\t"
      "s_waitcnt lgkmcnt(1)\n\t"
      "v_mfma_f32_32x32x16_bf16 v[64:79], v[204:207], v[192:195], v[160:175]\n\t"
      "v_exp_f32 v96, v96\n\t"
      "v_exp_f32 v97, v97\n\t"
      "v_exp_f32 v98, v98\n\t"
      "v_exp_f32 v99, v99\n\t"
      "v_smfmac_f32_32x32x32_bf16 v[144:159], v[212:215], v[88:95], v216\n\t"
      "ds_read_b128 v[212:215], v221 offset:4480\n\t"
      "v_exp_f32 v100, v100\n\t"
      "v_exp_f32 v101, v101\n\t"
      "v_exp_f32 v102, v102\n\t"
      "v_exp_f32 v103, v103\n\t"
      "s_mov_b32 %[t0], 110592\n\t"
      "buffer_load_dwordx4 v[8:11], v232, %[r1], %[t0] offen\n\t"
      "v_mfma_f32_32x32x16_bf16 v[80:95], v[204:207], v[196:199], v[176:191]\n\t"
      "v_exp_f32 v104, v104\n\t"
      "v_exp_f32 v105, v105\n\t"
      "v_exp_f32 v106, v106\n\t"
      "v_exp_f32 v107, v107\n\t"
      "s_waitcnt lgkmcnt(1)\n\t"
      "v_smfmac_f32_32x32x32_bf16 v[128:143], v[208:211], v[96:103], v216\n\t"
      "ds_read_b128 v[204:207], v217 offset:41984\n\t"
      "v_exp_f32 v108, v108\n\t"
      "v_exp_f32 v109, v109\n\t"
      "v_exp_f32 v110, v110\n\t"
      "v_exp_f32 v111, v111\n\t"
      "v_mfma_f32_32x32x4_2b_bf16 v[64:95], v[202:203], v[200:201], v[64:95] cbsz:1 abid:0\n\t"
      "v_exp_f32 v112, v112\n\t"
      "v_exp_f32 v113, v113\n\t"
      "v_exp_f32 v114, v114\n\t"
      "v_exp_f32 v115, v115\n\t"
      "s_mov_b32 m0, %[lo]\n\t"
      "s_add_u32 %[lo], %[lo], %[los]\n\t"
      "s_mov_b32 %[t0], 40960\n\t"
      "buffer_load_dwordx4 v227, %[ro], %[t0] offen lds\n\t"
      "v_exp_f32 v116, v116\n\t"
      "v_exp_f32 v117, v117\n\t"
      "v_exp_f32 v118, v118\n\t"
      "v_exp_f32 v119, v119\n\t"
      "s_waitcnt lgkmcnt(1)\n\t"
      "v_smfmac_f32_32x32x32_bf16 v[128:143], v[212:215], v[104:111], v216\n\t"
      "v_exp_f32 v120, v120\n\t"
      "v_exp_f32 v121, v121\n\t"
      "v_exp_f32 v122, v122\n\t"
      "v_exp_f32 v123, v123\n\t"
      "v_smfmac_f32_32x32x32_bf16 v[144:159], v[208:211], v[112:119], v216\n\t"
      "ds_read_b128 v[208:211], v222 offset:0\n\t"
      "v_exp_f32 v124, v124\n\t"
      "v_exp_f32 v125, v125\n\t"
      "v_exp_f32 v126, v126\n\t"
      "v_exp_f32 v127, v127\n\t"
      "s_setprio 3\n\t"
      "s_waitcnt lgkmcnt(1)\n\t"
      "v_mfma_f32_32x32x16_bf16 v[96:111], v[204:207], v[192:195], v[160:175]\n\t"
      "v_exp_f32 v64, v64\n\t"
      "s_waitcnt vmcnt(4)\n\t"
      "v_mov_b32 v225, 3\n\t"
      "v_exp_f32 v65, v65\n\t"
      "ds_write_b32 v223, v225\n\t"
      "v_exp_f32 v66, v66\n\t"
      "v_exp_f32 v67, v67\n\t"
      "v_smfmac_f32_32x32x32_bf16 v[144:159], v[212:215], v[120:127], v216\n\t"
      "ds_read_b128 v[212:215], v222 offset:640\n\t"
      "v_exp_f32 v68, v68\n\t"
      "v_exp_f32 v69, v69\n\t"
      "v_exp_f32 v70, v70\n\t"
      "v_exp_f32 v71, v71\n\t"
      "s_mov_b32 %[t0], 774144\n\t"
      "buffer_load_dwordx4 v[12:15], v232, %[r1], %[t0] offen\n\t"
      "v_mfma_f32_32x32x16_bf16 v[112:127], v[204:207], v[196:199], v[176:191]\n\t"
      "v_exp_f32 v72, v72\n\t"
      "v_exp_f32 v73, v73\n\t"
      "v_exp_f32 v74, v74\n\t"
      "v_exp_f32 v75, v75\n\t"
      "s_waitcnt lgkmcnt(1)\n\t"
      "v_smfmac_f32_32x32x32_bf16 v[128:143], v[208:211], v[64:71], v216\n\t"
      "ds_read_b128 v[204:207], v217 offset:43520\n\t"
      "v_exp_f32 v76, v76\n\t"
      "v_exp_f32 v77, v77\n\t"
      "v_exp_f32 v78, v78\n\t"
      "v_exp_f32 v79, v79\n\t"
      "v_mfma_f32_32x32x4_2b_bf16 v[96:127], v[202:203], v[200:201], v[96:127] cbsz:1 abid:1\n\t"
      "ds_read_b64 v[202:203], v218 offset:43520\n\t"
      "v_exp_f32 v80, v80\n\t"
      "v_exp_f32 v81, v81\n\t"
      "v_exp_f32 v82, v82\n\t"
      "v_exp_f32 v83, v83\n\t"
      "v_exp_f32 v84, v84\n\t"
      "v_exp_f32 v85, v85\n\t"
      "v_exp_f32 v86, v86\n\t"
      "v_exp_f32 v87, v87\n\t"
      "s_waitcnt lgkmcnt(2)\n\t"
      "v_smfmac_f32_32x32x32_bf16 v[128:143], v[212:215], v[72:79], v216\n\t"
      "v_exp_f32 v88, v88\n\t"
      "v_exp_f32 v89, v89\n\t"
      "v_exp_f32 v90, v90\n\t"
      "v_exp_f32 v91, v91\n\t"
      "v_smfmac_f32_32x32x32_bf16 v[144:159], v[208:211], v[80:87], v216\n\t"
      "ds_read_b128 v[208:211], v222 offset:1280\n\t"
      "v_exp_f32 v92, v92\n\t"
      "v_exp_f32 v93, v93\n\t"
      "v_exp_f32 v94, v94\n\t"
      "v_exp_f32 v95, v95\n\t"
      "s_setprio 2\n\t"
      "s_waitcnt lgkmcnt(1)\n\t"
      "v_mfma_f32_32x32x16_bf16 v[64:79], v[204:207], v[192:195], v[160:175]\n\t"
      "v_exp_f32 v96, v96\n\t"
      "v_exp_f32 v97, v97\n\t"
      "v_exp_f32 v98, v98\n\t"
      "v_exp_f32 v99, v99\n\t"
      "v_smfmac_f32_32x32x32_bf16 v[144:159], v[212:215], v[88:95], v216\n\t"
      "ds_read_b128 v[212:215], v222 offset:1920\n\t"
      "v_exp_f32 v100, v100\n\t"
      "v_exp_f32 v101, v101\n\t"
      "v_exp_f32 v102, v102\n\t"
      "v_exp_f32 v103, v103\n\t"
      "s_mov_b32 m0, %[lo]\n\t"
      "s_add_u32 %[lo], %[lo], %[los]\n\t"
      "s_mov_b32 %[t0], 81920\n\t"
      "buffer_load_dwordx4 v227, %[ro], %[t0] offen lds\n\t"
      "v_mfma_f32_32x32x16_bf16 v[80:95], v[204:207], v[196:199], v[176:191]\n\t"
      "v_exp_f32 v104, v104\n\t"
      "v_exp_f32 v105, v105\n\t"
      "v_exp_f32 v106, v106\n\t"
      "v_exp_f32 v107, v107\n\t"
      "s_waitcnt lgkmcnt(1)\n\t"
      "v_smfmac_f32_32x32x32_bf16 v[128:143], v[208:211], v[96:103], v216\n\t"
      "ds_read_b128 v[204:207], v217 offset:44544\n\t"
      "v_exp_f32 v108, v108\n\t"
      "v_exp_f32 v109, v109\n\t"
      "v_exp_f32 v110, v110\n\t"
      "v_exp_f32 v111, v111\n\t"
      "v_mfma_f32_32x32x4_2b_bf16 v[64:95], v[202:203], v[200:201], v[64:95] cbsz:1 abid:0\n\t"
      "v_exp_f32 v112, v112\n\t"
      "v_exp_f32 v113, v113\n\t"
      "v_exp_f32 v114, v114\n\t"
      "v_exp_f32 v115, v115\n\t"
      "s_mov_b32 %[t0], 884736\n\t"
      "buffer_load_dwordx4 v[16:19], v232, %[r1], %[t0] offen\n\t"
      "v_exp_f32 v116, v116\n\t"
      "v_exp_f32 v117, v117\n\t"
      "v_exp_f32 v118, v118\n\t"
      "v_exp_f32 v119, v119\n\t"
      "s_waitcnt lgkmcnt(1)\n\t"
      "v_smfmac_f32_32x32x32_bf16 v[128:143], v[212:215], v[104:111], v216\n\t"
      "ds_read_b32 v226, v224\n\t"
      "v_exp_f32 v120, v120\n\t"
      "v_exp_f32 v121, v121\n\t"
      "v_exp_f32 v122, v122\n\t"
      "v_exp_f32 v123, v123\n\t"
      "v_smfmac_f32_32x32x32_bf16 v[144:159], v[208:211], v[112:119], v216\n\t"
      "ds_read_b128 v[208:211], v222 offset:2560\n\t"
      "v_exp_f32 v124, v124\n\t"
      "v_exp_f32 v125, v125\n\t"
      "v_exp_f32 v126, v126\n\t"
      "v_exp_f32 v127, v127\n\t"
      "s_setprio 1\n\t"
      "s_waitcnt lgkmcnt(1)\n\t"
      "v_cmp_gt_u32 vcc, 3, v226\n\t"
      "s_cbranch_vccz 31f\n\t"
      "30:\n\t"
      "ds_read_b32 v226, v224\n\t"
      "s_waitcnt lgkmcnt(0)\n\t"
      "v_cmp_gt_u32 vcc, 3, v226\n\t"
      "s_cbranch_vccnz 30b\n\t"
      "31:\n\t"
      "ds_read_b64_tr_b16 v[228:229], v236\n\t"
      "ds_read_b64_tr_b16 v[230:231], v236 offset:5120\n\t"
      "ds_read_b64_tr_b16 v[238:239], v236 offset:256\n\t"
      "ds_read_b64_tr_b16 v[240:241], v236 offset:5376\n\t"
      "v_mfma_f32_32x32x16_bf16 v[96:111], v[204:207], v[192:195], v[160:175]\n\t"
      "v_exp_f32 v64, v64\n\t"
      "v_exp_f32 v65, v65\n\t"
      "s_cmp_lt_u32 %[wv], 4\n\t"
      "s_cbranch_scc1 32f\n\t"
      "s_add_u32 m0, %[lw], 32768\n\t"
      "s_add_u32 %[t0], %[gw], 94208\n\t"
      "buffer_load_dwordx4 v219, %[rs], %[t0] offen sc1 lds\n\t"
      "32:\n\t"
      "s_add_u32 m0, %[lw], 20480\n\t"
      "s_add_u32 %[t0], %[gw], 81920\n\t"
      "buffer_load_dwordx4 v219, %[rs], %[t0] offen sc1 lds\n\t"
      "v_exp_f32 v66, v66\n\t"
      "v_exp_f32 v67, v67\n\t"
      "v_smfmac_f32_32x32x32_bf16 v[144:159], v[212:215], v[120:127], v216\n\t"
      "ds_read_b128 v[212:215], v222 offset:3200\n\t"
      "v_exp_f32 v68, v68\n\t"
      "v_exp_f32 v69, v69\n\t"
      "s_add_u32 m0, %[lw], 28672\n\t"
      "s_add_u32 %[t0], %[gw], 90112\n\t"
      "buffer_load_dwordx4 v219, %[rs], %[t0] offen sc1 lds\n\t"
      "v_exp_f32 v70, v70\n\t"
      "v_exp_f32 v71, v71\n\t"
      "s_mov_b32 m0, %[lo]\n\t"
      "s_add_u32 %[lo], %[lo], %[los]\n\t"
      "s_mov_b32 %[t0], 122880\n\t"
      "buffer_load_dwordx4 v227, %[ro], %[t0] offen lds\n\t"
      "v_mfma_f32_32x32x16_bf16 v[112:127], v[204:207], v[196:199], v[176:191]\n\t"
      "v_exp_f32 v72, v72\n\t"
      "v_exp_f32 v73, v73\n\t"
      "v_exp_f32 v74, v74\n\t"
      "v_exp_f32 v75, v75\n\t"
      "s_waitcnt lgkmcnt(1)\n\t"
      "v_smfmac_f32_32x32x32_bf16 v[128:143], v[208:211], v[64:71], v216\n\t"
      "ds_read_b128 v[204:207], v217 offset:0\n\t"
      "v_exp_f32 v76, v76\n\t"
      "v_exp_f32 v77, v77\n\t"
      "buffer_store_dwordx4 v[228:231], v219, %[rs], %[d2a] offen sc1\n\t"
      "v_exp_f32 v78, v78\n\t"
      "v_exp_f32 v79, v79\n\t"
      "v_mfma_f32_32x32x4_2b_bf16 v[96:127], v[202:203], v[200:201], v[96:127] cbsz:1 abid:1\n\t"
      "ds_read_b64 v[202:203], v218 offset:0\n\t"
      "v_exp_f32 v80, v80\n\t"
      "v_exp_f32 v81, v81\n\t"
      "v_exp_f32 v82, v82\n\t"
      "v_exp_f32 v83, v83\n\t"
      "s_mov_b32 %[t0], 995328\n\t"
      "buffer_load_dwordx4 v[20:23], v232, %[r1], %[t0] offen\n\t"
      "v_exp_f32 v84, v84\n\t"
      "v_exp_f32 v85, v85\n\t"
      "v_exp_f32 v86, v86\n\t"
      "v_exp_f32 v87, v87\n\t"
      "buffer_store_dwordx4 v[238:241], v219, %[rs], %[d2b] offen sc1\n\t"
      "s_waitcnt lgkmcnt(2)\n\t"
      "v_smfmac_f32_32x32x32_bf16 v[128:143], v[212:215], v[72:79], v216\n\t"
      "v_exp_f32 v88, v88\n\t"
      "v_exp_f32 v89, v89\n\t"
      "v_exp_f32 v90, v90\n\t"
      "v_exp_f32 v91, v91\n\t"
      "v_smfmac_f32_32x32x32_bf16 v[144:159], v[208:211], v[80:87], v216\n\t"
      "ds_read_b128 v[208:211], v222 offset:3840\n\t"
      "v_exp_f32 v92, v92\n\t"
      "v_exp_f32 v93, v93\n\t"
      "v_exp_f32 v94, v94\n\t"
      "v_exp_f32 v95, v95\n\t"
      "s_setprio 0\n\t"
      "s_waitcnt lgkmcnt(1)\n\t"
      "v_mfma_f32_32x32x16_bf16 v[64:79], v[204:207], v[192:195], v[160:175]\n\t"
      "v_exp_f32 v96, v96\n\t"
      "v_exp_f32 v97, v97\n\t"
      "v_exp_f32 v98, v98\n\t"
      "v_exp_f32 v99, v99\n\t"
      "v_smfmac_f32_32x32x32_bf16 v[144:159], v[212:215], v[88:95], v216\n\t"
      "ds_read_b128 v[212:215], v222 offset:4480\n\t"
      "v_exp_f32 v100, v100\n\t"
      "v_exp_f32 v101, v101\n\t"
      "v_exp_f32 v102, v102\n\t"
      "v_exp_f32 v103, v103\n\t"
      "s_mov_b32 m0, %[lo]\n\t"
      "s_add_u32 %[lo], %[lo], %[los]\n\t"
      "s_mov_b32 %[t0], 163840\n\t"
      "buffer_load_dwordx4 v227, %[ro], %[t0] offen lds\n\t"
      "v_mfma_f32_32x32x16_bf16 v[80:95], v[204:207], v[196:199], v[176:191]\n\t"
      "v_exp_f32 v104, v104\n\t"
      "v_exp_f32 v105, v105\n\t"
      "v_exp_f32 v106, v106\n\t"
      "v_exp_f32 v107, v107\n\t"
      "s_waitcnt lgkmcnt(1)\n\t"
      "v_smfmac_f32_32x32x32_bf16 v[128:143], v[208:211], v[96:103], v216\n\t"
      "ds_read_b128 v[204:207], v217 offset:1024\n\t"
      "v_exp_f32 v108, v108\n\t"
      "v_exp_f32 v109, v109\n\t"
      "v_exp_f32 v110, v110\n\t"
      "v_exp_f32 v111, v111\n\t"
      "v_mfma_f32_32x32x4_2b_bf16 v[64:95], v[202:203], v[200:201], v[64:95] cbsz:1 abid:0\n\t"
      "v_exp_f32 v112, v112\n\t"
      "v_exp_f32 v113, v113\n\t"
      "v_exp_f32 v114, v114\n\t"
      "v_exp_f32 v115, v115\n\t"
      "v_exp_f32 v116, v116\n\t"
      "v_exp_f32 v117, v117\n\t"
      "v_exp_f32 v118, v118\n\t"
      "v_exp_f32 v119, v119\n\t"
      "s_waitcnt lgkmcnt(1)\n\t"
      "v_smfmac_f32_32x32x32_bf16 v[128:143], v[212:215], v[104:111], v216\n\t"
      "v_exp_f32 v120, v120\n\t"
      "v_exp_f32 v121, v121\n\t"
      "v_exp_f32 v122, v122\n\t"
      "v_exp_f32 v123, v123\n\t"
      "v_smfmac_f32_32x32x32_bf16 v[144:159], v[208:211], v[112:119], v216\n\t"
      "ds_read_b128 v[208:211], v220 offset:0\n\t"
      "v_exp_f32 v124, v124\n\t"
      "v_exp_f32 v125, v125\n\t"
      "v_exp_f32 v126, v126\n\t"
      "v_exp_f32 v127, v127\n\t"
      "s_setprio 3\n\t"
      "s_waitcnt lgkmcnt(1)\n\t"
      "v_mfma_f32_32x32x16_bf16 v[96:111], v[204:207], v[192:195], v[160:175]\n\t"
      "v_exp_f32 v64, v64\n\t"
      "s_waitcnt vmcnt(5)\n\t"
      "v_mov_b32 v225, 4\n\t"
      "v_exp_f32 v65, v65\n\t"
      "ds_write_b32 v223, v225\n\t"
      "v_exp_f32 v66, v66\n\t"
      "v_exp_f32 v67, v67\n\t"
      "v_smfmac_f32_32x32x32_bf16 v[144:159], v[212:215], v[120:127], v216\n\t"
      "ds_read_b128 v[212:215], v220 offset:640\n\t"
      "v_exp_f32 v68, v68\n\t"
      "v_exp_f32 v69, v69\n\t"
      "v_exp_f32 v70, v70\n\t"
      "v_exp_f32 v71, v71\n\t"
      "s_mov_b32 %[t0], 1105920\n\t"
      "buffer_load_dwordx4 v[24:27], v232, %[r1], %[t0] offen\n\t"
      "v_mfma_f32_32x32x16_bf16 v[112:127], v[204:207], v[196:199], v[176:191]\n\t"
      "v_exp_f32 v72, v72\n\t"
      "v_exp_f32 v73, v73\n\t"
      "v_exp_f32 v74, v74\n\t"
      "v_exp_f32 v75, v75\n\t"
      "s_waitcnt lgkmcnt(1)\n\t"
      "v_smfmac_f32_32x32x32_bf16 v[128:143], v[208:211], v[64:71], v216\n\t"
      "ds_read_b128 v[204:207], v217 offset:2560\n\t"
      "v_exp_f32 v76, v76\n\t"
      "v_exp_f32 v77, v77\n\t"
      "v_exp_f32 v78, v78\n\t"
      "v_exp_f32 v79, v79\n\t"
      "v_mfma_f32_32x32x4_2b_bf16 v[96:127], v[202:203], v[200:201], v[96:127] cbsz:1 abid:1\n\t"
      "ds_read_b64 v[202:203], v218 offset:2560\n\t"
      "v_exp_f32 v80, v80\n\t"
      "v_exp_f32 v81, v81\n\t"
      "v_exp_f32 v82, v82\n\t"
      "v_exp_f32 v83, v83\n\t"
      "s_mov_b32 m0, %[lo]\n\t"
      "s_add_u32 %[lo], %[lo], %[los]\n\t"
      "s_mov_b32 %[t0], 204800\n\t"
      "buffer_load_dwordx4 v227, %[ro], %[t0] offen lds\n\t"
      "v_exp_f32 v84, v84\n\t"
      "v_exp_f32 v85, v85\n\t"
      "v_exp_f32 v86, v86\n\t"
      "v_exp_f32 v87, v87\n\t"
      "s_waitcnt lgkmcnt(2)\n\t"
      "v_smfmac_f32_32x32x32_bf16 v[128:143], v[212:215], v[72:79], v216\n\t"
      "v_exp_f32 v88, v88\n\t"
      "v_exp_f32 v89, v89\n\t"
      "v_exp_f32 v90, v90\n\t"
      "v_exp_f32 v91, v91\n\t"
      "v_smfmac_f32_32x32x32_bf16 v[144:159], v[208:211], v[80:87], v216\n\t"
      "ds_read_b128 v[208:211], v220 offset:1280\n\t"
      "v_exp_f32 v92, v92\n\t"
      "v_exp_f32 v93, v93\n\t"
      "v_exp_f32 v94, v94\n\t"
      "v_exp_f32 v95, v95\n\t"
      "s_setprio 2\n\t"
      "s_waitcnt lgkmcnt(1)\n\t"
      "v_mfma_f32_32x32x16_bf16 v[64:79], v[204:207], v[192:195], v[160:175]\n\t"
      "v_exp_f32 v96, v96\n\t"
      "v_exp_f32 v97, v97\n\t"
      "v_exp_f32 v98, v98\n\t"
      "v_exp_f32 v99, v99\n\t"
      "v_smfmac_f32_32x32x32_bf16 v[144:159], v[212:215], v[88:95], v216\n\t"
      "ds_read_b128 v[212:215], v220 offset:1920\n\t"
      "v_exp_f32 v100, v100\n\t"
      "v_exp_f32 v101, v101\n\t"
      "v_exp_f32 v102, v102\n\t"
      "v_exp_f32 v103, v103\n\t"
      "s_mov_b32 %[t0], 1216512\n\t"
      "buffer_load_dwordx4 v[28:31], v232, %[r1], %[t0] offen\n\t"
      "v_mfma_f32_32x32x16_bf16 v[80:95], v[204:207], v[196:199], v[176:191]\n\t"
      "v_exp_f32 v104, v104\n\t"
      "v_exp_f32 v105, v105\n\t"
      "v_exp_f32 v106, v106\n\t"
      "v_exp_f32 v107, v107\n\t"
      "s_waitcnt lgkmcnt(1)\n\t"
      "v_smfmac_f32_32x32x32_bf16 v[128:143], v[208:211], v[96:103], v216\n\t"
      "ds_read_b128 v[204:207], v217 offset:3584\n\t"
      "v_exp_f32 v108, v108\n\t"
      "v_exp_f32 v109, v109\n\t"
      "v_exp_f32 v110, v110\n\t"
      "v_exp_f32 v111, v111\n\t"
      "v_mfma_f32_32x32x4_2b_bf16 v[64:95], v[202:203], v[200:201], v[64:95] cbsz:1 abid:0\n\t"
      "v_exp_f32 v112, v112\n\t"
      "v_exp_f32 v113, v113\n\t"
      "v_exp_f32 v114, v114\n\t"
      "v_exp_f32 v115, v115\n\t"
      "s_mov_b32 m0, %[lo]\n\t"
      "s_add_u32 %[lo], %[lo], %[los]\n\t"
      "s_mov_b32 %[t0], 245760\n\t"
      "buffer_load_dwordx4 v227, %[ro], %[t0] offen lds\n\t"
      "v_exp_f32 v116, v116\n\t"
      "v_exp_f32 v117, v117\n\t"
      "v_exp_f32 v118, v118\n\t"
      "v_exp_f32 v119, v119\n\t"
      "s_waitcnt lgkmcnt(1)\n\t"
      "v_smfmac_f32_32x32x32_bf16 v[128:143], v[212:215], v[104:111], v216\n\t"
      "ds_read_b32 v226, v224\n\t"
      "v_exp_f32 v120, v120\n\t"
      "v_exp_f32 v121, v121\n\t"
      "v_exp_f32 v122, v122\n\t"
      "v_exp_f32 v123, v123\n\t"
      "v_smfmac_f32_32x32x32_bf16 v[144:159], v[208:211], v[112:119], v216\n\t"
      "ds_read_b128 v[208:211], v220 offset:2560\n\t"
      "v_exp_f32 v124, v124\n\t"
      "v_exp_f32 v125, v125\n\t"
      "v_exp_f32 v126, v126\n\t"
      "v_exp_f32 v127, v127\n\t"
      "s_setprio 1\n\t"
      "s_waitcnt lgkmcnt(1)\n\t"
      "v_cmp_gt_u32 vcc, 4, v226\n\t"
      "s_cbranch_vccz 34f\n\t"
      "33:\n\t"
      "ds_read_b32 v226, v224\n\t"
      "s_waitcnt lgkmcnt(0)\n\t"
      "v_cmp_gt_u32 vcc, 4, v226\n\t"
      "s_cbranch_vccnz 33b\n\t"
      "34:\n\t"
      "v_mfma_f32_32x32x16_bf16 v[96:111], v[204:207], v[192:195], v[160:175]\n\t"
      "v_exp_f32 v64, v64\n\t"
      "v_exp_f32 v65, v65\n\t"
      "s_cmp_lt_u32 %[wv], 4\n\t"
      "s_cbranch_scc1 35f\n\t"
      "s_add_u32 m0, %[lw], 53248\n\t"
      "s_add_u32 %[t0], %[gw], 114688\n\t"
      "buffer_load_dwordx4 v219, %[rs], %[t0] offen sc1 lds\n\t"
      "35:\n\t"
      "s_add_u32 m0, %[lw], 40960\n\t"
      "s_add_u32 %[t0], %[gw], 102400\n\t"
      "buffer_load_dwordx4 v219, %[rs], %[t0] offen sc1 lds\n\t"
      "v_exp_f32 v66, v66\n\t"
      "v_exp_f32 v67, v67\n\t"
      "v_smfmac_f32_32x32x32_bf16 v[144:159], v[212:215], v[120:127], v216\n\t"
      "ds_read_b128 v[212:215], v220 offset:3200\n\t"
      "v_exp_f32 v68, v68\n\t"
      "v_exp_f32 v69, v69\n\t"
      "s_add_u32 m0, %[lw], 49152\n\t"
      "s_add_u32 %[t0], %[gw], 110592\n\t"
      "buffer_load_dwordx4 v219, %[rs], %[t0] offen sc1 lds\n\t"
      "v_exp_f32 v70, v70\n\t"
      "v_exp_f32 v71, v71\n\t"
      "s_mov_b32 %[t0], 1327104\n\t"
      "buffer_load_dwordx4 v[32:35], v232, %[r1], %[t0] offen\n\t"
      "v_mfma_f32_32x32x16_bf16 v[112:127], v[204:207], v[196:199], v[176:191]\n\t"
      "v_exp_f32 v72, v72\n\t"
      "v_exp_f32 v73, v73\n\t"
      "v_exp_f32 v74, v74\n\t"
      "v_exp_f32 v75, v75\n\t"
      "s_waitcnt lgkmcnt(1)\n\t"
      "v_smfmac_f32_32x32x32_bf16 v[128:143], v[208:211], v[64:71], v216\n\t"
      "ds_read_b128 v[204:207], v217 offset:20480\n\t"
      "v_exp_f32 v76, v76\n\t"
      "v_exp_f32 v77, v77\n\t"
      "v_exp_f32 v78, v78\n\t"
      "v_exp_f32 v79, v79\n\t"
      "v_mfma_f32_32x32x4_2b_bf16 v[96:127], v[202:203], v[200:201], v[96:127] cbsz:1 abid:1\n\t"
      "ds_read_b64 v[202:203], v218 offset:20480\n\t"
      "v_exp_f32 v80, v80\n\t"
      "v_exp_f32 v81, v81\n\t"
      "v_exp_f32 v82, v82\n\t"
      "v_exp_f32 v83, v83\n\t"
      "v_exp_f32 v84, v84\n\t"
      "v_exp_f32 v85, v85\n\t"
      "v_exp_f32 v86, v86\n\t"
      "v_exp_f32 v87, v87\n\t"
      "s_waitcnt lgkmcnt(2)\n\t"
      "v_smfmac_f32_32x32x32_bf16 v[128:143], v[212:215], v[72:79], v216\n\t"
      "v_exp_f32 v88, v88\n\t"
      "v_exp_f32 v89, v89\n\t"
      "v_exp_f32 v90, v90\n\t"
      "v_exp_f32 v91, v91\n\t"
      "v_smfmac_f32_32x32x32_bf16 v[144:159], v[208:211], v[80:87], v216\n\t"
      "ds_read_b128 v[208:211], v220 offset:3840\n\t"
      "v_exp_f32 v92, v92\n\t"
      "v_exp_f32 v93, v93\n\t"
      "v_exp_f32 v94, v94\n\t"
      "v_exp_f32 v95, v95\n\t"
      "s_setprio 0\n\t"
      "s_waitcnt lgkmcnt(1)\n\t"
      "v_mfma_f32_32x32x16_bf16 v[64:79], v[204:207], v[192:195], v[160:175]\n\t"
      "v_exp_f32 v96, v96\n\t"
      "v_exp_f32 v97, v97\n\t"
      "v_exp_f32 v98, v98\n\t"
      "v_exp_f32 v99, v99\n\t"
      "v_smfmac_f32_32x32x32_bf16 v[144:159], v[212:215], v[88:95], v216\n\t"
      "ds_read_b128 v[212:215], v220 offset:4480\n\t"
      "v_exp_f32 v100, v100\n\t"
      "v_exp_f32 v101, v101\n\t"
      "v_exp_f32 v102, v102\n\t"
      "v_exp_f32 v103, v103\n\t"
      "s_mov_b32 m0, %[lo]\n\t"
      "s_add_u32 %[lo], %[lo], %[los]\n\t"
      "s_mov_b32 %[t0], 286720\n\t"
      "buffer_load_dwordx4 v227, %[ro], %[t0] offen lds\n\t"
      "v_mfma_f32_32x32x16_bf16 v[80:95], v[204:207], v[196:199], v[176:191]\n\t"
      "v_exp_f32 v104, v104\n\t"
      "v_exp_f32 v105, v105\n\t"
      "v_exp_f32 v106, v106\n\t"
      "v_exp_f32 v107, v107\n\t"
      "s_waitcnt lgkmcnt(1)\n\t"
      "v_smfmac_f32_32x32x32_bf16 v[128:143], v[208:211], v[96:103], v216\n\t"
      "ds_read_b128 v[204:207], v217 offset:21504\n\t"
      "v_exp_f32 v108, v108\n\t"
      "v_exp_f32 v109, v109\n\t"
      "v_exp_f32 v110, v110\n\t"
      "v_exp_f32 v111, v111\n\t"
      "v_mfma_f32_32x32x4_2b_bf16 v[64:95], v[202:203], v[200:201], v[64:95] cbsz:1 abid:0\n\t"
      "v_exp_f32 v112, v112\n\t"
      "v_exp_f32 v113, v113\n\t"
      "v_exp_f32 v114, v114\n\t"
      "v_exp_f32 v115, v115\n\t"
      "s_mov_b32 %[t0], 1437696\n\t"
      "buffer_load_dwordx4 v[36:39], v232, %[r1], %[t0] offen\n\t"
      "v_exp_f32 v116, v116\n\t"
      "v_exp_f32 v117, v117\n\t"
      "v_exp_f32 v118, v118\n\t"
      "v_exp_f32 v119, v119\n\t"
      "s_waitcnt lgkmcnt(1)\n\t"
      "v_smfmac_f32_32x32x32_bf16 v[128:143], v[212:215], v[104:111], v216\n\t"
      "v_exp_f32 v120, v120\n\t"
      "v_exp_f32 v121, v121\n\t"
      "v_exp_f32 v122, v122\n\t"
      "v_exp_f32 v123, v123\n\t"
      "v_smfmac_f32_32x32x32_bf16 v[144:159], v[208:211], v[112:119], v216\n\t"
      "ds_read_b128 v[208:211], v221 offset:0\n\t"
      "v_exp_f32 v124, v124\n\t"
      "v_exp_f32 v125, v125\n\t"
      "v_exp_f32 v126, v126\n\t"
      "v_exp_f32 v127, v127\n\t"
      "s_setprio 3\n\t"
      "s_waitcnt lgkmcnt(1)\n\t"
      "v_mfma_f32_32x32x16_bf16 v[96:111], v[204:207], v[192:195], v[160:175]\n\t"
      "v_exp_f32 v64, v64\n\t"
      "s_waitcnt vmcnt(3)\n\t"
      "v_mov_b32 v225, 5\n\t"
      "v_exp_f32 v65, v65\n\t"
      "ds_write_b32 v223, v225\n\t"
      "v_exp_f32 v66, v66\n\t"
      "v_exp_f32 v67, v67\n\t"
      "v_smfmac_f32_32x32x32_bf16 v[144:159], v[212:215], v[120:127], v216\n\t"
      "ds_read_b128 v[212:215], v221 offset:640\n\t"
      "v_exp_f32 v68, v68\n\t"
      "v_exp_f32 v69, v69\n\t"
      "v_exp_f32 v70, v70\n\t"
      "v_exp_f32 v71, v71\n\t"
      "s_mov_b32 m0, %[lo]\n\t"
      "s_add_u32 %[lo], %[lo], %[los]\n\t"
      "s_mov_b32 %[t0], 327680\n\t"
      "buffer_load_dwordx4 v227, %[ro], %[t0] offen lds\n\t"
      "v_mfma_f32_32x32x16_bf16 v[112:127], v[204:207], v[196:199], v[176:191]\n\t"
      "v_exp_f32 v72, v72\n\t"
      "v_exp_f32 v73, v73\n\t"
      "v_exp_f32 v74, v74\n\t"
      "v_exp_f32 v75, v75\n\t"
      "s_waitcnt lgkmcnt(1)\n\t"
      "v_smfmac_f32_32x32x32_bf16 v[128:143], v[208:211], v[64:71], v216\n\t"
      "ds_read_b128 v[204:207], v217 offset:23040\n\t"
      "v_exp_f32 v76, v76\n\t"
      "v_exp_f32 v77, v77\n\t"
      "v_exp_f32 v78, v78\n\t"
      "v_exp_f32 v79, v79\n\t"
      "v_mfma_f32_32x32x4_2b_bf16 v[96:127], v[202:203], v[200:201], v[96:127] cbsz:1 abid:1\n\t"
      "ds_read_b64 v[202:203], v218 offset:23040\n\t"
      "v_exp_f32 v80, v80\n\t"
      "v_exp_f32 v81, v81\n\t"
      "v_exp_f32 v82, v82\n\t"
      "v_exp_f32 v83, v83\n\t"
      "s_mov_b32 %[t0], 1548288\n\t"
      "buffer_load_dwordx4 v[40:43], v232, %[r1], %[t0] offen\n\t"
      "v_exp_f32 v84, v84\n\t"
      "v_exp_f32 v85, v85\n\t"
      "v_exp_f32 v86, v86\n\t"
      "v_exp_f32 v87, v87\n\t"
      "s_waitcnt lgkmcnt(2)\n\t"
      "v_smfmac_f32_32x32x32_bf16 v[128:143], v[212:215], v[72:79], v216\n\t"
      "v_exp_f32 v88, v88\n\t"
      "v_exp_f32 v89, v89\n\t"
      "v_exp_f32 v90, v90\n\t"
      "v_exp_f32 v91, v91\n\t"
      "v_smfmac_f32_32x32x32_bf16 v[144:159], v[208:211], v[80:87], v216\n\t"
      "ds_read_b128 v[208:211], v221 offset:1280\n\t"
      "v_exp_f32 v92, v92\n\t"
      "v_exp_f32 v93, v93\n\t"
      "v_exp_f32 v94, v94\n\t"
      "v_exp_f32 v95, v95\n\t"
      "s_setprio 2\n\t"
      "s_waitcnt lgkmcnt(1)\n\t"
      "v_mfma_f32_32x32x16_bf16 v[64:79], v[204:207], v[192:195], v[160:175]\n\t"
      "v_exp_f32 v96, v96\n\t"
      "v_exp_f32 v97, v97\n\t"
      "v_exp_f32 v98, v98\n\t"
      "v_exp_f32 v99, v99\n\t"
      "v_smfmac_f32_32x32x32_bf16 v[144:159], v[212:215], v[88:95], v216\n\t"
      "ds_read_b128 v[212:215], v221 offset:1920\n\t"
      "v_exp_f32 v100, v100\n\t"
      "v_exp_f32 v101, v101\n\t"
      "v_exp_f32 v102, v102\n\t"
      "v_exp_f32 v103, v103\n\t"
      "s_mov_b32 m0, %[lo]\n\t"
      "s_add_u32 %[lo], %[lo], %[los]\n\t"
      "s_mov_b32 %[t0], 368640\n\t"
      "buffer_load_dwordx4 v227, %[ro], %[t0] offen lds\n\t"
      "v_mfma_f32_32x32x16_bf16 v[80:95], v[204:207], v[196:199], v[176:191]\n\t"
      "v_exp_f32 v104, v104\n\t"
      "v_exp_f32 v105, v105\n\t"
      "v_exp_f32 v106, v106\n\t"
      "v_exp_f32 v107, v107\n\t"
      "s_waitcnt lgkmcnt(1)\n\t"
      "v_smfmac_f32_32x32x32_bf16 v[128:143], v[208:211], v[96:103], v216\n\t"
      "ds_read_b128 v[204:207], v217 offset:24064\n\t"
      "v_exp_f32 v108, v108\n\t"
      "v_exp_f32 v109, v109\n\t"
      "v_exp_f32 v110, v110\n\t"
      "v_exp_f32 v111, v111\n\t"
      "v_mfma_f32_32x32x4_2b_bf16 v[64:95], v[202:203], v[200:201], v[64:95] cbsz:1 abid:0\n\t"
      "v_exp_f32 v112, v112\n\t"
      "v_exp_f32 v113, v113\n\t"
      "v_exp_f32 v114, v114\n\t"
      "v_exp_f32 v115, v115\n\t"
      "v_exp_f32 v116, v116\n\t"
      "v_exp_f32 v117, v117\n\t"
      "v_exp_f32 v118, v118\n\t"
      "v_exp_f32 v119, v119\n\t"
      "s_waitcnt lgkmcnt(1)\n\t"
      "v_smfmac_f32_32x32x32_bf16 v[128:143], v[212:215], v[104:111], v216\n\t"
      "ds_read_b32 v226, v224\n\t"
      "v_exp_f32 v120, v120\n\t"
      "v_exp_f32 v121, v121\n\t"
      "v_exp_f32 v122, v122\n\t"
      "v_exp_f32 v123, v123\n\t"
      "v_smfmac_f32_32x32x32_bf16 v[144:159], v[208:211], v[112:119], v216\n\t"
      "ds_read_b128 v[208:211], v221 offset:2560\n\t"
      "v_exp_f32 v124, v124\n\t"
      "v_exp_f32 v125, v125\n\t"
      "v_exp_f32 v126, v126\n\t"
      "v_exp_f32 v127, v127\n\t"
      "s_setprio 1\n\t"
      "s_waitcnt lgkmcnt(1)\n\t"
      "v_cmp_gt_u32 vcc, 5, v226\n\t"
      "s_cbranch_vccz 37f\n\t"
      "36:\n\t"
      "ds_read_b32 v226, v224\n\t"
      "s_waitcnt lgkmcnt(0)\n\t"
      "v_cmp_gt_u32 vcc, 5, v226\n\t"
      "s_cbranch_vccnz 36b\n\t"
      "37:\n\t"
      "v_mfma_f32_32x32x16_bf16 v[96:111], v[204:207], v[192:195], v[160:175]\n\t"
      "v_exp_f32 v64, v64\n\t"
      "s_cmp_eq_u32 %[wv], 7\n\t"
      "s_cbranch_scc0 39f\n\t"
      "v_mov_b32 v228, %[tg0]\n\t"
      "v_mov_b32 v229, %[tg1]\n\t"
      "buffer_store_dwordx2 v[228:229], off, %[rs], %[fw] sc1\n\t"
      "39:\n\t"
      "v_exp_f32 v65, v65\n\t"
      "s_cmp_lt_u32 %[wv], 4\n\t"
      "s_cbranch_scc1 38f\n\t"
      "s_add_u32 m0, %[lw], 12288\n\t"
      "s_add_u32 %[t0], %[gw], 135168\n\t"
      "buffer_load_dwordx4 v219, %[rs], %[t0] offen sc1 lds\n\t"
      "38:\n\t"
      "s_add_u32 m0, %[lw], 0\n\t"
      "s_add_u32 %[t0], %[gw], 122880\n\t"
      "buffer_load_dwordx4 v219, %[rs], %[t0] offen sc1 lds\n\t"
      "v_exp_f32 v66, v66\n\t"
      "v_exp_f32 v67, v67\n\t"
      "v_smfmac_f32_32x32x32_bf16 v[144:159], v[212:215], v[120:127], v216\n\t"
      "ds_read_b128 v[212:215], v221 offset:3200\n\t"
      "v_exp_f32 v68, v68\n\t"
      "v_exp_f32 v69, v69\n\t"
      "s_add_u32 m0, %[lw], 8192\n\t"
      "s_add_u32 %[t0], %[gw], 131072\n\t"
      "buffer_load_dwordx4 v219, %[rs], %[t0] offen sc1 lds\n\t"
      "v_exp_f32 v70, v70\n\t"
      "v_exp_f32 v71, v71\n\t"
      "s_mov_b32 %[t0], 1658880\n\t"
      "buffer_load_dwordx4 v[44:47], v232, %[r1], %[t0] offen\n\t"
      "v_mfma_f32_32x32x16_bf16 v[112:127], v[204:207], v[196:199], v[176:191]\n\t"
      "v_exp_f32 v72, v72\n\t"
      "v_exp_f32 v73, v73\n\t"
      "v_exp_f32 v74, v74\n\t"
      "v_exp_f32 v75, v75\n\t"
      "s_waitcnt lgkmcnt(1)\n\t"
      "v_smfmac_f32_32x32x32_bf16 v[128:143], v[208:211], v[64:71], v216\n\t"
      "ds_read_b128 v[204:207], v217 offset:40960\n\t"
      "v_exp_f32 v76, v76\n\t"
      "v_exp_f32 v77, v77\n\t"
      "v_exp_f32 v78, v78\n\t"
      "v_exp_f32 v79, v79\n\t"
      "v_mfma_f32_32x32x4_2b_bf16 v[96:127], v[202:203], v[200:201], v[96:127] cbsz:1 abid:1\n\t"
      "ds_read_b64 v[202:203], v218 offset:40960\n\t"
      "v_exp_f32 v80, v80\n\t"
      "v_exp_f32 v81, v81\n\t"
      "v_exp_f32 v82, v82\n\t"
      "v_exp_f32 v83, v83\n\t"
      "s_mov_b32 m0, %[lo]\n\t"
      "s_add_u32 %[lo], %[lo], %[los]\n\t"
      "s_mov_b32 %[t0], 409600\n\t"
      "buffer_load_dwordx4 v227, %[ro], %[t0] offen lds\n\t"
      "v_exp_f32 v84, v84\n\t"
      "v_exp_f32 v85, v85\n\t"
      "v_exp_f32 v86, v86\n\t"
      "v_exp_f32 v87, v87\n\t"
      "s_waitcnt lgkmcnt(2)\n\t"
      "v_smfmac_f32_32x32x32_bf16 v[128:143], v[212:215], v[72:79], v216\n\t"
      "v_exp_f32 v88, v88\n\t"
      "v_exp_f32 v89, v89\n\t"
      "v_exp_f32 v90, v90\n\t"
      "v_exp_f32 v91, v91\n\t"
      "v_smfmac_f32_32x32x32_bf16 v[144:159], v[208:211], v[80:87], v216\n\t"
      "ds_read_b128 v[208:211], v221 offset:3840\n\t"
      "v_exp_f32 v92, v92\n\t"
      "v_exp_f32 v93, v93\n\t"
      "v_exp_f32 v94, v94\n\t"
      "v_exp_f32 v95, v95\n\t"
      "s_setprio 0\n\t"
      "s_waitcnt lgkmcnt(1)\n\t"
      "v_mfma_f32_32x32x16_bf16 v[64:79], v[204:207], v[192:195], v[160:175]\n\t"
      "v_exp_f32 v96, v96\n\t"
      "v_exp_f32 v97, v97\n\t"
      "v_exp_f32 v98, v98\n\t"
      "v_exp_f32 v99, v99\n\t"
      "v_smfmac_f32_32x32x32_bf16 v[144:159], v[212:215], v[88:95], v216\n\t"
      "ds_read_b128 v[212:215], v221 offset:4480\n\t"
      "v_exp_f32 v100, v100\n\t"
      "v_exp_f32 v101, v101\n\t"
      "v_exp_f32 v102, v102\n\t"
      "v_exp_f32 v103, v103\n\t"
      "s_mov_b32 %[t0], 1769472\n\t"
      "buffer_load_dwordx4 v[48:51], v232, %[r1], %[t0] offen\n\t"
      "v_mfma_f32_32x32x16_bf16 v[80:95], v[204:207], v[196:199], v[176:191]\n\t"
      "v_exp_f32 v104, v104\n\t"
      "v_exp_f32 v105, v105\n\t"
      "v_exp_f32 v106, v106\n\t"
      "v_exp_f32 v107, v107\n\t"
      "s_waitcnt lgkmcnt(1)\n\t"
      "v_smfmac_f32_32x32x32_bf16 v[128:143], v[208:211], v[96:103], v216\n\t"
      "ds_read_b128 v[204:207], v217 offset:41984\n\t"
      "v_exp_f32 v108, v108\n\t"
      "v_exp_f32 v109, v109\n\t"
      "v_exp_f32 v110, v110\n\t"
      "v_exp_f32 v111, v111\n\t"
      "v_mfma_f32_32x32x4_2b_bf16 v[64:95], v[202:203], v[200:201], v[64:95] cbsz:1 abid:0\n\t"
      "v_exp_f32 v112, v112\n\t"
      "v_exp_f32 v113, v113\n\t"
      "v_exp_f32 v114, v114\n\t"
      "v_exp_f32 v115, v115\n\t"
      "s_mov_b32 m0, %[lo]\n\t"
      "s_add_u32 %[lo], %[lo], %[los]\n\t"
      "s_mov_b32 %[t0], 450560\n\t"
      "buffer_load_dwordx4 v227, %[ro], %[t0] offen lds\n\t"
      "v_exp_f32 v116, v116\n\t"
      "v_exp_f32 v117, v117\n\t"
      "v_exp_f32 v118, v118\n\t"
      "v_exp_f32 v119, v119\n\t"
      "s_waitcnt lgkmcnt(1)\n\t"
      "v_smfmac_f32_32x32x32_bf16 v[128:143], v[212:215], v[104:111], v216\n\t"
      "v_exp_f32 v120, v120\n\t"
      "v_exp_f32 v121, v121\n\t"
      "v_exp_f32 v122, v122\n\t"
      "v_exp_f32 v123, v123\n\t"
      "v_smfmac_f32_32x32x32_bf16 v[144:159], v[208:211], v[112:119], v216\n\t"
      "ds_read_b128 v[208:211], v222 offset:0\n\t"
      "v_exp_f32 v124, v124\n\t"
      "v_exp_f32 v125, v125\n\t"
      "v_exp_f32 v126, v126\n\t"
      "v_exp_f32 v127, v127\n\t"
      "s_setprio 3\n\t"
      "s_waitcnt lgkmcnt(1)\n\t"
      "v_mfma_f32_32x32x16_bf16 v[96:111], v[204:207], v[192:195], v[160:175]\n\t"
      "v_exp_f32 v64, v64\n\t"
      "s_waitcnt vmcnt(4)\n\t"
      "v_mov_b32 v225, 6\n\t"
      "v_exp_f32 v65, v65\n\t"
      "ds_write_b32 v223, v225\n\t"
      "v_exp_f32 v66, v66\n\t"
      "v_exp_f32 v67, v67\n\t"
      "v_smfmac_f32_32x32x32_bf16 v[144:159], v[212:215], v[120:127], v216\n\t"
      "ds_read_b128 v[212:215], v222 offset:640\n\t"
      "v_exp_f32 v68, v68\n\t"
      "v_exp_f32 v69, v69\n\t"
      "v_exp_f32 v70, v70\n\t"
      "v_exp_f32 v71, v71\n\t"
      "s_mov_b32 %[t0], 1880064\n\t"
      "buffer_load_dwordx4 v[52:55], v232, %[r1], %[t0] offen\n\t"
      "v_mfma_f32_32x32x16_bf16 v[112:127], v[204:207], v[196:199], v[176:191]\n\t"
      "v_exp_f32 v72, v72\n\t"
      "v_exp_f32 v73, v73\n\t"
      "v_exp_f32 v74, v74\n\t"
      "v_exp_f32 v75, v75\n\t"
      "s_waitcnt lgkmcnt(1)\n\t"
      "v_smfmac_f32_32x32x32_bf16 v[128:143], v[208:211], v[64:71], v216\n\t"
      "ds_read_b128 v[204:207], v217 offset:43520\n\t"
      "v_exp_f32 v76, v76\n\t"
      "v_exp_f32 v77, v77\n\t"
      "v_exp_f32 v78, v78\n\t"
      "v_exp_f32 v79, v79\n\t"
      "v_mfma_f32_32x32x4_2b_bf16 v[96:127], v[202:203], v[200:201], v[96:127] cbsz:1 abid:1\n\t"
      "ds_read_b64 v[202:203], v218 offset:43520\n\t"
      "v_exp_f32 v80, v80\n\t"
      "v_exp_f32 v81, v81\n\t"
      "v_exp_f32 v82, v82\n\t"
      "v_exp_f32 v83, v83\n\t"
      "v_exp_f32 v84, v84\n\t"
      "v_exp_f32 v85, v85\n\t"
      "v_exp_f32 v86, v86\n\t"
      "v_exp_f32 v87, v87\n\t"
      "s_waitcnt lgkmcnt(2)\n\t"
      "v_smfmac_f32_32x32x32_bf16 v[128:143], v[212:215], v[72:79], v216\n\t"
      "v_exp_f32 v88, v88\n\t"
      "v_exp_f32 v89, v89\n\t"
      "v_exp_f32 v90, v90\n\t"
      "v_exp_f32 v91, v91\n\t"
      "v_smfmac_f32_32x32x32_bf16 v[144:159], v[208:211], v[80:87], v216\n\t"
      "ds_read_b128 v[208:211], v222 offset:1280\n\t"
      "v_exp_f32 v92, v92\n\t"
      "v_exp_f32 v93, v93\n\t"
      "v_exp_f32 v94, v94\n\t"
      "v_exp_f32 v95, v95\n\t"
      "s_setprio 2\n\t"
      "s_waitcnt lgkmcnt(1)\n\t"
      "v_mfma_f32_32x32x16_bf16 v[64:79], v[204:207], v[192:195], v[160:175]\n\t"
      "v_exp_f32 v96, v96\n\t"
      "v_exp_f32 v97, v97\n\t"
      "v_exp_f32 v98, v98\n\t"
      "v_exp_f32 v99, v99\n\t"
      "v_smfmac_f32_32x32x32_bf16 v[144:159], v[212:215], v[88:95], v216\n\t"
      "ds_read_b128 v[212:215], v222 offset:1920\n\t"
      "v_exp_f32 v100, v100\n\t"
      "v_exp_f32 v101, v101\n\t"
      "v_exp_f32 v102, v102\n\t"
      "v_exp_f32 v103, v103\n\t"
      "s_mov_b32 m0, %[lo]\n\t"
      "s_add_u32 %[lo], %[lo], %[los]\n\t"
      "s_mov_b32 %[t0], 491520\n\t"
      "buffer_load_dwordx4 v227, %[ro], %[t0] offen lds\n\t"
      "v_mfma_f32_32x32x16_bf16 v[80:95], v[204:207], v[196:199], v[176:191]\n\t"
      "v_exp_f32 v104, v104\n\t"
      "v_exp_f32 v105, v105\n\t"
      "v_exp_f32 v106, v106\n\t"
      "v_exp_f32 v107, v107\n\t"
      "s_waitcnt lgkmcnt(1)\n\t"
      "v_smfmac_f32_32x32x32_bf16 v[128:143], v[208:211], v[96:103], v216\n\t"
      "ds_read_b128 v[204:207], v217 offset:44544\n\t"
      "v_exp_f32 v108, v108\n\t"
      "v_exp_f32 v109, v109\n\t"
      "v_exp_f32 v110, v110\n\t"
      "v_exp_f32 v111, v111\n\t"
      "v_mfma_f32_32x32x4_2b_bf16 v[64:95], v[202:203], v[200:201], v[64:95] cbsz:1 abid:0\n\t"
      "v_exp_f32 v112, v112\n\t"
      "v_exp_f32 v113, v113\n\t"
      "v_exp_f32 v114, v114\n\t"
      "v_exp_f32 v115, v115\n\t"
      "s_mov_b32 %[t0], 1990656\n\t"
      "buffer_load_dwordx4 v[56:59], v232, %[r1], %[t0] offen\n\t"
      "v_exp_f32 v116, v116\n\t"
      "v_exp_f32 v117, v117\n\t"
      "v_exp_f32 v118, v118\n\t"
      "v_exp_f32 v119, v119\n\t"
      "s_waitcnt lgkmcnt(1)\n\t"
      "v_smfmac_f32_32x32x32_bf16 v[128:143], v[212:215], v[104:111], v216\n\t"
      "ds_read_b32 v226, v224\n\t"
      "v_exp_f32 v120, v120\n\t"
      "v_exp_f32 v121, v121\n\t"
      "v_exp_f32 v122, v122\n\t"
      "v_exp_f32 v123, v123\n\t"
      "v_smfmac_f32_32x32x32_bf16 v[144:159], v[208:211], v[112:119], v216\n\t"
      "ds_read_b128 v[208:211], v222 offset:2560\n\t"
      "v_exp_f32 v124, v124\n\t"
      "v_exp_f32 v125, v125\n\t"
      "v_exp_f32 v126, v126\n\t"
      "v_exp_f32 v127, v127\n\t"
      "s_setprio 1\n\t"
      "s_waitcnt lgkmcnt(1)\n\t"
      "v_cmp_gt_u32 vcc, 6, v226\n\t"
      "s_cbranch_vccz 41f\n\t"
      "40:\n\t"
      "ds_read_b32 v226, v224\n\t"
      "s_waitcnt lgkmcnt(0)\n\t"
      "v_cmp_gt_u32 vcc, 6, v226\n\t"
      "s_cbranch_vccnz 40b\n\t"
      "41:\n\t"
      "v_mfma_f32_32x32x16_bf16 v[96:111], v[204:207], v[192:195], v[160:175]\n\t"
      "v_exp_f32 v64, v64\n\t"
      "v_exp_f32 v65, v65\n\t"
      "s_cmp_lt_u32 %[wv], 4\n\t"
      "s_cbranch_scc1 42f\n\t"
      "s_add_u32 m0, %[lw], 32768\n\t"
      "s_add_u32 %[t0], %[gw], 155648\n\t"
      "buffer_load_dwordx4 v219, %[rs], %[t0] offen sc1 lds\n\t"
      "42:\n\t"
      "s_add_u32 m0, %[lw], 20480\n\t"
      "s_add_u32 %[t0], %[gw], 143360\n\t"
      "buffer_load_dwordx4 v219, %[rs], %[t0] offen sc1 lds\n\t"
      "v_exp_f32 v66, v66\n\t"
      "v_exp_f32 v67, v67\n\t"
      "v_smfmac_f32_32x32x32_bf16 v[144:159], v[212:215], v[120:127], v216\n\t"
      "ds_read_b128 v[212:215], v222 offset:3200\n\t"
      "v_exp_f32 v68, v68\n\t"
      "v_exp_f32 v69, v69\n\t"
      "s_add_u32 m0, %[lw], 28672\n\t"
      "s_add_u32 %[t0], %[gw], 151552\n\t"
      "buffer_load_dwordx4 v219, %[rs], %[t0] offen sc1 lds\n\t"
      "v_exp_f32 v70, v70\n\t"
      "v_exp_f32 v71, v71\n\t"
      "s_mov_b32 m0, %[lo]\n\t"
      "s_add_u32 %[lo], %[lo], %[los]\n\t"
      "s_mov_b32 %[t0], 532480\n\t"
      "buffer_load_dwordx4 v227, %[ro], %[t0] offen lds\n\t"
      "v_mfma_f32_32x32x16_bf16 v[112:127], v[204:207], v[196:199], v[176:191]\n\t"
      "v_exp_f32 v72, v72\n\t"
      "v_exp_f32 v73, v73\n\t"
      "v_exp_f32 v74, v74\n\t"
      "v_exp_f32 v75, v75\n\t"
      "s_waitcnt lgkmcnt(1)\n\t"
      "v_smfmac_f32_32x32x32_bf16 v[128:143], v[208:211], v[64:71], v216\n\t"
      "ds_read_b128 v[204:207], v217 offset:0\n\t"
      "v_exp_f32 v76, v76\n\t"
      "v_exp_f32 v77, v77\n\t"
      "s_add_u32 %[t0], %[g2], 262144\n\t"
      "buffer_load_dwordx4 v[228:231], v219, %[rs], %[t0] offen\n\t"
      "v_exp_f32 v78, v78\n\t"
      "v_exp_f32 v79, v79\n\t"
      "v_mfma_f32_32x32x4_2b_bf16 v[96:127], v[202:203], v[200:201], v[96:127] cbsz:1 abid:1\n\t"
      "ds_read_b64 v[202:203], v218 offset:0\n\t"
      "v_exp_f32 v80, v80\n\t"
      "v_exp_f32 v81, v81\n\t"
      "v_exp_f32 v82, v82\n\t"
      "v_exp_f32 v83, v83\n\t"
      "s_mov_b32 %[t0], 2101248\n\t"
      "buffer_load_dwordx4 v[60:63], v232, %[r1], %[t0] offen\n\t"
      "v_exp_f32 v84, v84\n\t"
      "v_exp_f32 v85, v85\n\t"
      "v_exp_f32 v86, v86\n\t"
      "v_exp_f32 v87, v87\n\t"
      "s_waitcnt lgkmcnt(2)\n\t"
      "v_smfmac_f32_32x32x32_bf16 v[128:143], v[212:215], v[72:79], v216\n\t"
      "v_exp_f32 v88, v88\n\t"
      "v_exp_f32 v89, v89\n\t"
      "v_exp_f32 v90, v90\n\t"
      "v_exp_f32 v91, v91\n\t"
      "s_add_u32 %[t0], %[g2], 0\n\t"
      "buffer_load_dwordx4 v[228:231], v219, %[rs], %[t0] offen\n\t"
      "v_smfmac_f32_32x32x32_bf16 v[144:159], v[208:211], v[80:87], v216\n\t"
      "ds_read_b128 v[208:211], v222 offset:3840\n\t"
      "v_exp_f32 v92, v92\n\t"
      "v_exp_f32 v93, v93\n\t"
      "v_exp_f32 v94, v94\n\t"
      "v_exp_f32 v95, v95\n\t"
      "s_setprio 0\n\t"
      "s_waitcnt lgkmcnt(1)\n\t"
      "v_mfma_f32_32x32x16_bf16 v[64:79], v[204:207], v[192:195], v[160:175]\n\t"
      "v_exp_f32 v96, v96\n\t"
      "v_exp_f32 v97, v97\n\t"
      "v_exp_f32 v98, v98\n\t"
      "v_exp_f32 v99, v99\n\t"
      "v_smfmac_f32_32x32x32_bf16 v[144:159], v[212:215], v[88:95], v216\n\t"
      "ds_read_b128 v[212:215], v222 offset:4480\n\t"
      "v_exp_f32 v100, v100\n\t"
      "v_exp_f32 v101, v101\n\t"
      "v_exp_f32 v102, v102\n\t"
      "v_exp_f32 v103, v103\n\t"
      "s_mov_b32 m0, %[lo]\n\t"
      "s_add_u32 %[lo], %[lo], %[los]\n\t"
      "s_mov_b32 %[t0], 573440\n\t"
      "buffer_load_dwordx4 v227, %[ro], %[t0] offen lds\n\t"
      "v_mfma_f32_32x32x16_bf16 v[80:95], v[204:207], v[196:199], v[176:191]\n\t"
      "v_exp_f32 v104, v104\n\t"
      "v_exp_f32 v105, v105\n\t"
      "v_exp_f32 v106, v106\n\t"
      "v_exp_f32 v107, v107\n\t"
      "s_waitcnt lgkmcnt(1)\n\t"
      "v_smfmac_f32_32x32x32_bf16 v[128:143], v[208:211], v[96:103], v216\n\t"
      "ds_read_b128 v[204:207], v217 offset:1024\n\t"
      "v_exp_f32 v108, v108\n\t"
      "v_exp_f32 v109, v109\n\t"
      "v_exp_f32 v110, v110\n\t"
      "v_exp_f32 v111, v111\n\t"
      "v_mfma_f32_32x32x4_2b_bf16 v[64:95], v[202:203], v[200:201], v[64:95] cbsz:1 abid:0\n\t"
      "v_exp_f32 v112, v112\n\t"
      "v_exp_f32 v113, v113\n\t"
      "v_exp_f32 v114, v114\n\t"
      "v_exp_f32 v115, v115\n\t"
      "s_add_u32 %[t0], %[g2], 524288\n\t"
      "buffer_load_dwordx4 v[228:231], v219, %[rs], %[t0] offen\n\t"
      "v_exp_f32 v116, v116\n\t"
      "v_exp_f32 v117, v117\n\t"
      "v_exp_f32 v118, v118\n\t"
      "v_exp_f32 v119, v119\n\t"
      "s_waitcnt lgkmcnt(1)\n\t"
      "v_smfmac_f32_32x32x32_bf16 v[128:143], v[212:215], v[104:111], v216\n\t"
      "v_exp_f32 v120, v120\n\t"
      "v_exp_f32 v121, v121\n\t"
      "v_exp_f32 v122, v122\n\t"
      "v_exp_f32 v123, v123\n\t"
      "v_smfmac_f32_32x32x32_bf16 v[144:159], v[208:211], v[112:119], v216\n\t"
      "ds_read_b128 v[208:211], v220 offset:0\n\t"
      "v_exp_f32 v124, v124\n\t"
      "v_exp_f32 v125, v125\n\t"
      "v_exp_f32 v126, v126\n\t"
      "v_exp_f32 v127, v127\n\t"
      "s_setprio 3\n\t"
      "s_waitcnt lgkmcnt(1)\n\t"
      "v_mfma_f32_32x32x16_bf16 v[96:111], v[204:207], v[192:195], v[160:175]\n\t"
      "v_exp_f32 v64, v64\n\t"
      "s_waitcnt vmcnt(6)\n\t"
      "v_mov_b32 v225, 7\n\t"
      "v_exp_f32 v65, v65\n\t"
      "ds_write_b32 v223, v225\n\t"
      "v_exp_f32 v66, v66\n\t"
      "v_exp_f32 v67, v67\n\t"
      "v_smfmac_f32_32x32x32_bf16 v[144:159], v[212:215], v[120:127], v216\n\t"
      "ds_read_b128 v[212:215], v220 offset:640\n\t"
      "v_exp_f32 v68, v68\n\t"
      "v_exp_f32 v69, v69\n\t"
      "v_exp_f32 v70, v70\n\t"
      "v_exp_f32 v71, v71\n\t"
      "s_mov_b32 m0, %[lo]\n\t"
      "s_add_u32 %[lo], %[lo], %[los]\n\t"
      "s_mov_b32 %[t0], 614400\n\t"
      "buffer_load_dwordx4 v227, %[ro], %[t0] offen lds\n\t"
      "v_mfma_f32_32x32x16_bf16 v[112:127], v[204:207], v[196:199], v[176:191]\n\t"
      "v_exp_f32 v72, v72\n\t"
      "v_exp_f32 v73, v73\n\t"
      "v_exp_f32 v74, v74\n\t"
      "v_exp_f32 v75, v75\n\t"
      "s_waitcnt lgkmcnt(1)\n\t"
      "v_smfmac_f32_32x32x32_bf16 v[128:143], v[208:211], v[64:71], v216\n\t"
      "ds_read_b128 v[204:207], v217 offset:2560\n\t"
      "v_exp_f32 v76, v76\n\t"
      "v_exp_f32 v77, v77\n\t"
      "v_exp_f32 v78, v78\n\t"
      "v_exp_f32 v79, v79\n\t"
      "v_mfma_f32_32x32x4_2b_bf16 v[96:127], v[202:203], v[200:201], v[96:127] cbsz:1 abid:1\n\t"
      "ds_read_b64 v[202:203], v218 offset:2560\n\t"
      "v_exp_f32 v80, v80\n\t"
      "v_exp_f32 v81, v81\n\t"
      "v_exp_f32 v82, v82\n\t"
      "v_exp_f32 v83, v83\n\t"
      "s_mov_b32 m0, %[lo]\n\t"
      "s_add_u32 %[lo], %[lo], %[los]\n\t"
      "s_mov_b32 %[t0], 655360\n\t"
      "buffer_load_dwordx4 v227, %[ro], %[t0] offen lds\n\t"
      "v_exp_f32 v84, v84\n\t"
      "v_exp_f32 v85, v85\n\t"
      "v_exp_f32 v86, v86\n\t"
      "v_exp_f32 v87, v87\n\t"
      "s_waitcnt lgkmcnt(2)\n\t"
      "v_smfmac_f32_32x32x32_bf16 v[128:143], v[212:215], v[72:79], v216\n\t"
      "v_exp_f32 v88, v88\n\t"
      "v_exp_f32 v89, v89\n\t"
      "v_exp_f32 v90, v90\n\t"
      "v_exp_f32 v91, v91\n\t"
      "s_add_u32 %[t0], %[g2], 786432\n\t"
      "buffer_load_dwordx4 v[228:231], v219, %[rs], %[t0] offen\n\t"
      "v_smfmac_f32_32x32x32_bf16 v[144:159], v[208:211], v[80:87], v216\n\t"
      "ds_read_b128 v[208:211], v220 offset:1280\n\t"
      "v_exp_f32 v92, v92\n\t"
      "v_exp_f32 v93, v93\n\t"
      "v_exp_f32 v94, v94\n\t"
      "v_exp_f32 v95, v95\n\t"
      "s_setprio 2\n\t"
      "s_waitcnt lgkmcnt(1)\n\t"
      "v_mfma_f32_32x32x16_bf16 v[64:79], v[204:207], v[192:195], v[160:175]\n\t"
      "v_exp_f32 v96, v96\n\t"
      "v_exp_f32 v97, v97\n\t"
      "v_exp_f32 v98, v98\n\t"
      "v_exp_f32 v99, v99\n\t"
      "v_smfmac_f32_32x32x32_bf16 v[144:159], v[212:215], v[88:95], v216\n\t"
      "ds_read_b128 v[212:215], v220 offset:1920\n\t"
      "v_exp_f32 v100, v100\n\t"
      "v_exp_f32 v101, v101\n\t"
      "v_exp_f32 v102, v102\n\t"
      "v_exp_f32 v103, v103\n\t"
      "s_mov_b32 m0, %[lo]\n\t"
      "s_add_u32 %[lo], %[lo], %[los]\n\t"
      "s_mov_b32 %[t0], 696320\n\t"
      "buffer_load_dwordx4 v227, %[ro], %[t0] offen lds\n\t"
      "v_mfma_f32_32x32x16_bf16 v[80:95], v[204:207], v[196:199], v[176:191]\n\t"
      "v_exp_f32 v104, v104\n\t"
      "v_exp_f32 v105, v105\n\t"
      "v_exp_f32 v106, v106\n\t"
      "v_exp_f32 v107, v107\n\t"
      "s_waitcnt lgkmcnt(1)\n\t"
      "v_smfmac_f32_32x32x32_bf16 v[128:143], v[208:211], v[96:103], v216\n\t"
      "ds_read_b128 v[204:207], v217 offset:3584\n\t"
      "v_exp_f32 v108, v108\n\t"
      "v_exp_f32 v109, v109\n\t"
      "v_exp_f32 v110, v110\n\t"
      "v_exp_f32 v111, v111\n\t"
      "v_mfma_f32_32x32x4_2b_bf16 v[64:95], v[202:203], v[200:201], v[64:95] cbsz:1 abid:0\n\t"
      "v_exp_f32 v112, v112\n\t"
      "v_exp_f32 v113, v113\n\t"
      "v_exp_f32 v114, v114\n\t"
      "v_exp_f32 v115, v115\n\t"
      "s_mov_b32 %[t0], 737280\n\t"
      "buffer_load_dwordx4 v[234:237], v227, %[ro], %[t0] offen\n\t"
      "v_exp_f32 v116, v116\n\t"
      "v_exp_f32 v117, v117\n\t"
      "v_exp_f32 v118, v118\n\t"
      "v_exp_f32 v119, v119\n\t"
      "s_waitcnt lgkmcnt(1)\n\t"
      "v_smfmac_f32_32x32x32_bf16 v[128:143], v[212:215], v[104:111], v216\n\t"
      "ds_read_b32 v226, v224\n\t"
      "v_exp_f32 v120, v120\n\t"
      "v_exp_f32 v121, v121\n\t"
      "v_exp_f32 v122, v122\n\t"
      "v_exp_f32 v123, v123\n\t"
      "s_add_u32 %[t0], %[g2], 1048576\n\t"
      "buffer_load_dwordx4 v[228:231], v219, %[rs], %[t0] offen\n\t"
      "v_smfmac_f32_32x32x32_bf16 v[144:159], v[208:211], v[112:119], v216\n\t"
      "ds_read_b128 v[208:211], v220 offset:2560\n\t"
      "v_exp_f32 v124, v124\n\t"
      "v_exp_f32 v125, v125\n\t"
      "v_exp_f32 v126, v126\n\t"
      "v_exp_f32 v127, v127\n\t"
      "s_setprio 1\n\t"
      "s_waitcnt lgkmcnt(1)\n\t"
      "v_cmp_gt_u32 vcc, 7, v226\n\t"
      "s_cbranch_vccz 44f\n\t"
      "43:\n\t"
      "ds_read_b32 v226, v224\n\t"
      "s_waitcnt lgkmcnt(0)\n\t"
      "v_cmp_gt_u32 vcc, 7, v226\n\t"
      "s_cbranch_vccnz 43b\n\t"
      "44:\n\t"
      "v_mfma_f32_32x32x16_bf16 v[96:111], v[204:207], v[192:195], v[160:175]\n\t"
      "v_exp_f32 v64, v64\n\t"
      "v_exp_f32 v65, v65\n\t"
      "v_exp_f32 v66, v66\n\t"
      "v_exp_f32 v67, v67\n\t"
      "v_smfmac_f32_32x32x32_bf16 v[144:159], v[212:215], v[120:127], v216\n\t"
      "ds_read_b128 v[212:215], v220 offset:3200\n\t"
      "v_exp_f32 v68, v68\n\t"
      "v_exp_f32 v69, v69\n\t"
      "v_exp_f32 v70, v70\n\t"
      "v_exp_f32 v71, v71\n\t"
      "s_mov_b32 %[t0], 778240\n\t"
      "buffer_load_dwordx4 v[238:241], v227, %[ro], %[t0] offen\n\t"
      "v_mfma_f32_32x32x16_bf16 v[112:127], v[204:207], v[196:199], v[176:191]\n\t"
      "v_exp_f32 v72, v72\n\t"
      "v_exp_f32 v73, v73\n\t"
      "v_exp_f32 v74, v74\n\t"
      "v_exp_f32 v75, v75\n\t"
      "s_waitcnt lgkmcnt(1)\n\t"
      "v_smfmac_f32_32x32x32_bf16 v[128:143], v[208:211], v[64:71], v216\n\t"
      "ds_read_b128 v[204:207], v217 offset:20480\n\t"
      "v_exp_f32 v76, v76\n\t"
      "v_exp_f32 v77, v77\n\t"
      "v_exp_f32 v78, v78\n\t"
      "v_exp_f32 v79, v79\n\t"
      "v_mfma_f32_32x32x4_2b_bf16 v[96:127], v[202:203], v[200:201], v[96:127] cbsz:1 abid:1\n\t"
      "ds_read_b64 v[202:203], v218 offset:20480\n\t"
      "v_exp_f32 v80, v80\n\t"
      "v_exp_f32 v81, v81\n\t"
      "v_exp_f32 v82, v82\n\t"
      "v_exp_f32 v83, v83\n\t"
      "s_add_u32 %[t0], %[g2], 1310720\n\t"
      "buffer_load_dwordx4 v[228:231], v219, %[rs], %[t0] offen\n\t"
      "v_exp_f32 v84, v84\n\t"
      "v_exp_f32 v85, v85\n\t"
      "v_exp_f32 v86, v86\n\t"
      "v_exp_f32 v87, v87\n\t"
      "s_waitcnt lgkmcnt(2)\n\t"
      "v_smfmac_f32_32x32x32_bf16 v[128:143], v[212:215], v[72:79], v216\n\t"
      "v_exp_f32 v88, v88\n\t"
      "v_exp_f32 v89, v89\n\t"
      "v_exp_f32 v90, v90\n\t"
      "v_exp_f32 v91, v91\n\t"
      "v_smfmac_f32_32x32x32_bf16 v[144:159], v[208:211], v[80:87], v216\n\t"
      "ds_read_b128 v[208:211], v220 offset:3840\n\t"
      "v_exp_f32 v92, v92\n\t"
      "v_exp_f32 v93, v93\n\t"
      "v_exp_f32 v94, v94\n\t"
      "v_exp_f32 v95, v95\n\t"
      "s_setprio 0\n\t"
      "s_waitcnt lgkmcnt(1)\n\t"
      "v_mfma_f32_32x32x16_bf16 v[64:79], v[204:207], v[192:195], v[160:175]\n\t"
      "v_exp_f32 v96, v96\n\t"
      "v_exp_f32 v97, v97\n\t"
      "v_exp_f32 v98, v98\n\t"
      "v_exp_f32 v99, v99\n\t"
      "v_smfmac_f32_32x32x32_bf16 v[144:159], v[212:215], v[88:95], v216\n\t"
      "ds_read_b128 v[212:215], v220 offset:4480\n\t"
      "v_exp_f32 v100, v100\n\t"
      "v_exp_f32 v101, v101\n\t"
      "v_exp_f32 v102, v102\n\t"
      "v_exp_f32 v103, v103\n\t"
      "s_add_u32 %[t0], %[g2], 1572864\n\t"
      "buffer_load_dwordx4 v[228:231], v219, %[rs], %[t0] offen\n\t"
      "v_mfma_f32_32x32x16_bf16 v[80:95], v[204:207], v[196:199], v[176:191]\n\t"
      "v_exp_f32 v104, v104\n\t"
      "v_exp_f32 v105, v105\n\t"
      "v_exp_f32 v106, v106\n\t"
      "v_exp_f32 v107, v107\n\t"
      "s_waitcnt lgkmcnt(1)\n\t"
      "v_smfmac_f32_32x32x32_bf16 v[128:143], v[208:211], v[96:103], v216\n\t"
      "ds_read_b128 v[204:207], v217 offset:21504\n\t"
      "v_exp_f32 v108, v108\n\t"
      "v_exp_f32 v109, v109\n\t"
      "v_exp_f32 v110, v110\n\t"
      "v_exp_f32 v111, v111\n\t"
      "v_mfma_f32_32x32x4_2b_bf16 v[64:95], v[202:203], v[200:201], v[64:95] cbsz:1 abid:0\n\t"
      "v_exp_f32 v112, v112\n\t"
      "v_exp_f32 v113, v113\n\t"
      "v_exp_f32 v114, v114\n\t"
      "v_exp_f32 v115, v115\n\t"
      "v_exp_f32 v116, v116\n\t"
      "v_exp_f32 v117, v117\n\t"
      "v_exp_f32 v118, v118\n\t"
      "v_exp_f32 v119, v119\n\t"
      "s_waitcnt lgkmcnt(1)\n\t"
      "v_smfmac_f32_32x32x32_bf16 v[128:143], v[212:215], v[104:111], v216\n\t"
      "v_exp_f32 v120, v120\n\t"
      "v_exp_f32 v121, v121\n\t"
      "v_exp_f32 v122, v122\n\t"
      "v_exp_f32 v123, v123\n\t"
      "v_smfmac_f32_32x32x32_bf16 v[144:159], v[208:211], v[112:119], v216\n\t"
      "ds_read_b128 v[208:211], v221 offset:0\n\t"
      "v_exp_f32 v124, v124\n\t"
      "v_exp_f32 v125, v125\n\t"
      "v_exp_f32 v126, v126\n\t"
      "v_exp_f32 v127, v127\n\t"
      "s_setprio 3\n\t"
      "s_waitcnt lgkmcnt(1)\n\t"
      "v_mfma_f32_32x32x16_bf16 v[96:111], v[204:207], v[192:195], v[160:175]\n\t"
      "v_exp_f32 v64, v64\n\t"
      "v_exp_f32 v65, v65\n\t"
      "v_exp_f32 v66, v66\n\t"
      "v_exp_f32 v67, v67\n\t"
      "v_smfmac_f32_32x32x32_bf16 v[144:159], v[212:215], v[120:127], v216\n\t"
      "ds_read_b128 v[212:215], v221 offset:640\n\t"
      "v_exp_f32 v68, v68\n\t"
      "v_exp_f32 v69, v69\n\t"
      "v_exp_f32 v70, v70\n\t"
      "v_exp_f32 v71, v71\n\t"
      "s_add_u32 %[t0], %[g2], 1835008\n\t"
      "buffer_load_dwordx4 v[228:231], v219, %[rs], %[t0] offen\n\t"
      "v_mfma_f32_32x32x16_bf16 v[112:127], v[204:207], v[196:199], v[176:191]\n\t"
      "v_exp_f32 v72, v72\n\t"
      "v_exp_f32 v73, v73\n\t"
      "v_exp_f32 v74, v74\n\t"
      "v_exp_f32 v75, v75\n\t"
      "s_waitcnt lgkmcnt(1)\n\t"
      "v_smfmac_f32_32x32x32_bf16 v[128:143], v[208:211], v[64:71], v216\n\t"
      "ds_read_b128 v[204:207], v217 offset:23040\n\t"
      "v_exp_f32 v76, v76\n\t"
      "v_exp_f32 v77, v77\n\t"
      "v_exp_f32 v78, v78\n\t"
      "v_exp_f32 v79, v79\n\t"
      "v_mfma_f32_32x32x4_2b_bf16 v[96:127], v[202:203], v[200:201], v[96:127] cbsz:1 abid:1\n\t"
      "ds_read_b64 v[202:203], v218 offset:23040\n\t"
      "v_exp_f32 v80, v80\n\t"
      "v_exp_f32 v81, v81\n\t"
      "v_exp_f32 v82, v82\n\t"
      "v_exp_f32 v83, v83\n\t"
      "buffer_load_dwordx4 v[228:231], v219, %[rc], %[co] offen\n\t"
      "v_exp_f32 v84, v84\n\t"
      "v_exp_f32 v85, v85\n\t"
      "v_exp_f32 v86, v86\n\t"
      "v_exp_f32 v87, v87\n\t"
      "s_waitcnt lgkmcnt(2)\n\t"
      "v_smfmac_f32_32x32x32_bf16 v[128:143], v[212:215], v[72:79], v216\n\t"
      "v_exp_f32 v88, v88\n\t"
      "v_exp_f32 v89, v89\n\t"
      "v_exp_f32 v90, v90\n\t"
      "v_exp_f32 v91, v91\n\t"
      "v_smfmac_f32_32x32x32_bf16 v[144:159], v[208:211], v[80:87], v216\n\t"
      "ds_read_b128 v[208:211], v221 offset:1280\n\t"
      "v_exp_f32 v92, v92\n\t"
      "v_exp_f32 v93, v93\n\t"
      "v_exp_f32 v94, v94\n\t"
      "v_exp_f32 v95, v95\n\t"
      "s_setprio 2\n\t"
      "s_waitcnt lgkmcnt(1)\n\t"
      "v_mfma_f32_32x32x16_bf16 v[64:79], v[204:207], v[192:195], v[160:175]\n\t"
      "v_exp_f32 v96, v96\n\t"
      "v_exp_f32 v97, v97\n\t"
      "v_exp_f32 v98, v98\n\t"
      "v_exp_f32 v99, v99\n\t"
      "v_smfmac_f32_32x32x32_bf16 v[144:159], v[212:215], v[88:95], v216\n\t"
      "ds_read_b128 v[212:215], v221 offset:1920\n\t"
      "v_exp_f32 v100, v100\n\t"
      "v_exp_f32 v101, v101\n\t"
      "v_exp_f32 v102, v102\n\t"
      "v_exp_f32 v103, v103\n\t"
      "s_add_u32 %[t0], %[g2], 2097152\n\t"
      "buffer_load_dwordx4 v[228:231], v219, %[rs], %[t0] offen\n\t"
      "v_mfma_f32_32x32x16_bf16 v[80:95], v[204:207], v[196:199], v[176:191]\n\t"
      "v_exp_f32 v104, v104\n\t"
      "v_exp_f32 v105, v105\n\t"
      "v_exp_f32 v106, v106\n\t"
      "v_exp_f32 v107, v107\n\t"
      "s_waitcnt lgkmcnt(1)\n\t"
      "v_smfmac_f32_32x32x32_bf16 v[128:143], v[208:211], v[96:103], v216\n\t"
      "ds_read_b128 v[204:207], v217 offset:24064\n\t"
      "v_exp_f32 v108, v108\n\t"
      "v_exp_f32 v109, v109\n\t"
      "v_exp_f32 v110, v110\n\t"
      "v_exp_f32 v111, v111\n\t"
      "v_mfma_f32_32x32x4_2b_bf16 v[64:95], v[202:203], v[200:201], v[64:95] cbsz:1 abid:0\n\t"
      "v_exp_f32 v112, v112\n\t"
      "v_exp_f32 v113, v113\n\t"
      "v_exp_f32 v114, v114\n\t"
      "v_exp_f32 v115, v115\n\t"
      "v_exp_f32 v116, v116\n\t"
      "v_exp_f32 v117, v117\n\t"
      "v_exp_f32 v118, v118\n\t"
      "v_exp_f32 v119, v119\n\t"
      "s_waitcnt lgkmcnt(1)\n\t"
      "v_smfmac_f32_32x32x32_bf16 v[128:143], v[212:215], v[104:111], v216\n\t"
      "v_exp_f32 v120, v120\n\t"
      "v_exp_f32 v121, v121\n\t"
      "v_exp_f32 v122, v122\n\t"
      "v_exp_f32 v123, v123\n\t"
      "v_smfmac_f32_32x32x32_bf16 v[144:159], v[208:211], v[112:119], v216\n\t"
      "ds_read_b128 v[208:211], v221 offset:2560\n\t"
      "v_exp_f32 v124, v124\n\t"
      "v_exp_f32 v125, v125\n\t"
      "v_exp_f32 v126, v126\n\t"
      "v_exp_f32 v127, v127\n\t"
      "s_setprio 1\n\t"
      "s_waitcnt lgkmcnt(1)\n\t"
      "v_mfma_f32_32x32x16_bf16 v[96:111], v[204:207], v[192:195], v[160:175]\n\t"
      "v_exp_f32 v64, v64\n\t"
      "v_exp_f32 v65, v65\n\t"
      "v_exp_f32 v66, v66\n\t"
      "v_exp_f32 v67, v67\n\t"
      "v_smfmac_f32_32x32x32_bf16 v[144:159], v[212:215], v[120:127], v216\n\t"
      "ds_read_b128 v[212:215], v221 offset:3200\n\t"
      "v_exp_f32 v68, v68\n\t"
      "v_exp_f32 v69, v69\n\t"
      "v_exp_f32 v70, v70\n\t"
      "v_exp_f32 v71, v71\n\t"
      "v_mfma_f32_32x32x16_bf16 v[112:127], v[204:207], v[196:199], v[176:191]\n\t"
      "v_exp_f32 v72, v72\n\t"
      "v_exp_f32 v73, v73\n\t"
      "v_exp_f32 v74, v74\n\t"
      "v_exp_f32 v75, v75\n\t"
      "s_waitcnt lgkmcnt(1)\n\t"
      "v_smfmac_f32_32x32x32_bf16 v[128:143], v[208:211], v[64:71], v216\n\t"
      "v_exp_f32 v76, v76\n\t"
      "v_exp_f32 v77, v77\n\t"
      "v_exp_f32 v78, v78\n\t"
      "v_exp_f32 v79, v79\n\t"
      "v_mfma_f32_32x32x4_2b_bf16 v[96:127], v[202:203], v[200:201], v[96:127] cbsz:1 abid:1\n\t"
      "v_exp_f32 v80, v80\n\t"
      "v_exp_f32 v81, v81\n\t"
      "v_exp_f32 v82, v82\n\t"
      "v_exp_f32 v83, v83\n\t"
      "v_exp_f32 v84, v84\n\t"
      "v_exp_f32 v85, v85\n\t"
      "v_exp_f32 v86, v86\n\t"
      "v_exp_f32 v87, v87\n\t"
      "s_waitcnt lgkmcnt(0)\n\t"
      "v_smfmac_f32_32x32x32_bf16 v[128:143], v[212:215], v[72:79], v216\n\t"
      "v_exp_f32 v88, v88\n\t"
      "v_exp_f32 v89, v89\n\t"
      "v_exp_f32 v90, v90\n\t"
      "v_exp_f32 v91, v91\n\t"
      "v_smfmac_f32_32x32x32_bf16 v[144:159], v[208:211], v[80:87], v216\n\t"
      "ds_read_b128 v[208:211], v221 offset:3840\n\t"
      "v_exp_f32 v92, v92\n\t"
      "v_exp_f32 v93, v93\n\t"
      "v_exp_f32 v94, v94\n\t"
      "v_exp_f32 v95, v95\n\t"
      "s_setprio 0\n\t"
      "s_waitcnt lgkmcnt(1)\n\t"
      "v_exp_f32 v96, v96\n\t"
      "v_exp_f32 v97, v97\n\t"
      "v_exp_f32 v98, v98\n\t"
      "v_exp_f32 v99, v99\n\t"
      "v_smfmac_f32_32x32x32_bf16 v[144:159], v[212:215], v[88:95], v216\n\t"
      "ds_read_b128 v[212:215], v221 offset:4480\n\t"
      "v_exp_f32 v100, v100\n\t"
      "v_exp_f32 v101, v101\n\t"
      "v_exp_f32 v102, v102\n\t"
      "v_exp_f32 v103, v103\n\t"
      "v_exp_f32 v104, v104\n\t"
      "v_exp_f32 v105, v105\n\t"
      "v_exp_f32 v106, v106\n\t"
      "v_exp_f32 v107, v107\n\t"
      "s_waitcnt lgkmcnt(1)\n\t"
      "v_smfmac_f32_32x32x32_bf16 v[128:143], v[208:211], v[96:103], v216\n\t"
      "v_exp_f32 v108, v108\n\t"
      "v_exp_f32 v109, v109\n\t"
      "v_exp_f32 v110, v110\n\t"
      "v_exp_f32 v111, v111\n\t"
      "v_exp_f32 v112, v112\n\t"
      "v_exp_f32 v113, v113\n\t"
      "v_exp_f32 v114, v114\n\t"
      "v_exp_f32 v115, v115\n\t"
      "v_exp_f32 v116, v116\n\t"
      "v_exp_f32 v117, v117\n\t"
      "v_exp_f32 v118, v118\n\t"
      "v_exp_f32 v119, v119\n\t"
      "s_waitcnt lgkmcnt(0)\n\t"
      "v_smfmac_f32_32x32x32_bf16 v[128:143], v[212:215], v[104:111], v216\n\t"
      "v_exp_f32 v120, v120\n\t"
      "v_exp_f32 v121, v121\n\t"
      "v_exp_f32 v122, v122\n\t"
      "v_exp_f32 v123, v123\n\t"
      "v_smfmac_f32_32x32x32_bf16 v[144:159], v[208:211], v[112:119], v216\n\t"
      "v_exp_f32 v124, v124\n\t"
      "v_exp_f32 v125, v125\n\t"
      "v_exp_f32 v126, v126\n\t"
      "v_exp_f32 v127, v127\n\t"
      "s_setprio 0\n\t"
      "s_nop 0\n\t"
      "v_smfmac_f32_32x32x32_bf16 v[144:159], v[212:215], v[120:127], v216\n\t"
      "s_waitcnt vmcnt(0) lgkmcnt(0)\n\t"
      "s_nop 15\n\t"
      "s_nop 3\n\t"
      : "={v[128:143]}"(oa), "={v[144:159]}"(ob), [t0] "=&s"(t0), [lo] "+s"(lo), "+{v242}"(ma), "+{v243}"(mb_), "={v[234:237]}"(wo2a), "={v[238:241]}"(wo2b),
        "={v[4:7]}"(w1r[0]), "={v[8:11]}"(w1r[1]), "={v[12:15]}"(w1r[2]), "={v[16:19]}"(w1r[3]), "={v[20:23]}"(w1r[4]), "={v[24:27]}"(w1r[5]), "={v[28:31]}"(w1r[6]), "={v[32:35]}"(w1r[7]), "={v[36:39]}"(w1r[8]), "={v[40:43]}"(w1r[9]), "={v[44:47]}"(w1r[10]), "={v[48:51]}"(w1r[11]), "={v[52:55]}"(w1r[12]), "={v[56:59]}"(w1r[13]), "={v[60:63]}"(w1r[14])
      : "{v216}"(idx), "{v217}"(ak), "{v218}"(akr), "{v219}"(vd),
        "{v220}"(av), "{v221}"(av1), "{v222}"(av2), "{v223}"(pw), "{v224}"(pr), "{v227}"(vwo), "{v232}"(vw1), "{v233}"(vfl), "{v234}"(t2a), "{v235}"(t2b), "{v236}"(t2r),
        [rs] "s"(rs), [ro] "s"(ro_), [r1] "s"(r1_), [wv] "s"(w), [lw] "s"(lw), [gw] "s"(gw), [los] "s"(los), [g2] "s"(g2),
        [qp] "s"(qp), [rc] "s"(rc_), [co] "s"(co), [qa] "s"(qa), [qb] "s"(qb), [qx] "s"(qx), [r2] "s"(r2_), [s2] "s"(s2), [l2] "s"(l2), [s2b] "s"(s2b), [l2b] "s"(l2b), [d2a] "s"(d2a), [d2b] "s"(d2b), [pass] "s"(pass), [fw] "s"(fw), [tg0] "s"((unsigned)tag), [tg1] "s"((unsigned)(tag >> 32))
      : "memory", "scc", "vcc",
        "v64", "v65", "v66", "v67", "v68", "v69", "v70", "v71", "v72", "v73", "v74", "v75", "v76", "v77", "v78", "v79", "v80", "v81", "v82", "v83", "v84", "v85", "v86", "v87", "v88", "v89", "v90", "v91", "v92", "v93", "v94", "v95", "v96", "v97", "v98", "v99", "v100", "v101", "v102", "v103", "v104", "v105", "v106", "v107", "v108", "v109", "v110", "v111", "v112", "v113", "v114", "v115", "v116", "v117", "v118", "v119", "v120", "v121", "v122", "v123", "v124", "v125", "v126", "v127", "v160", "v161", "v162", "v163", "v164", "v165", "v166", "v167", "v168", "v169", "v170", "v171", "v172", "v173", "v174", "v175", "v176", "v177", "v178", "v179", "v180", "v181", "v182", "v183", "v184", "v185", "v186", "v187", "v188", "v189", "v190", "v191", "v192", "v193", "v194", "v195", "v196", "v197", "v198", "v199", "v200", "v201", "v202", "v203", "v204", "v205", "v206", "v207", "v208", "v209", "v210", "v211", "v212", "v213", "v214", "v215", "v225", "v226", "v228", "v229", "v230", "v231");
    STAMP(7);
    l = lane_again(); hh = l >> 5; r = l & 31;
    auto x2 = [&](int v) { return (LAS f32x4*)(smem + (v < 4 ? L_XCH + 16384 + v * 1024 : L_XCH2 + (v - 4) * 1024) + l * 16); };
    LAS f32x4* xo = (LAS f32x4*)(smem + L_XCH + w * 2048 + l * 16), * xi = (LAS f32x4*)(smem + L_XCH + (w ^ 4) * 2048 + l * 16);
    xo[0] = (f32x4){ob[0], ob[1], ob[2], ob[3]}; xo[64] = (f32x4){ob[4], ob[5], ob[6], ob[7]};
    *x2(w) = (f32x4){ob[8], hh ? mb_ : ob[9], ob[10], ob[11]};
    WAITL_BAR();
    const f32x4 t0v = xi[0], t1v = xi[64], t2v = *x2(w ^ 4);
    const float mp = upper32(t2v[1]), mx = fmaxf(ma, mp), fm = __builtin_amdgcn_exp2f(ma - mx), fp = __builtin_amdgcn_exp2f(mp - mx);
    float o[12]; f32x2 nf = {0.f, 0.f};
#pragma unroll
    for (int e = 0; e < 12; ++e) o[e] = oa[e] * fm + (e < 4 ? t0v[e & 3] : e < 8 ? t1v[e & 3] : t2v[e & 3]) * fp;
#pragma unroll
    for (int e = 0; e < 12; e += 2) asm("v_pk_fma_f32 %0, %1, 0, %0 op_sel_hi:[1,0,1]" : "+v"(nf) : "v"((f32x2){o[e], o[e + 1]}));
    const bool bad = !(nf[0] + nf[1] == 0.f) || (hh && !(fmaxf(oa[8], t2v[0]) < 0x1p60f));
    if (bad && pass == 0) *(LAS unsigned*)(smem + L_FLAG) = 0x7FA5C3E1u;
    const float li = __builtin_amdgcn_rcpf(upper32(o[8]));
    const unsigned ob0 = WS_O + (qblk * 16 + qg * 4 + kh * 2 + (r >> 4)) * 20480;
    __builtin_amdgcn_raw_buffer_store_b128((u32x4){pk(o[0] * li, o[1] * li), pk(o[2] * li, o[3] * li), pk(o[4] * li, o[5] * li), pk(o[6] * li, o[7] * li)}, rs,
                                           ob0 + (head >> 1) * 1024 + ((2 * (head & 1) + hh) * 16 + (r & 15)) * 16, 0, SC1);
    if (!hh) __builtin_amdgcn_raw_buffer_store_b64((u32x2){pk(o[8] * li, o[9] * li), pk(o[10] * li, o[11] * li)}, rs, ob0 + 16384 + head * 128 + (r & 15) * 8, 0, SC1);
    SBAR();
    if (w < 5) { *(LAS u32x4*)(smem + L_WO2 + w * 1024 + l * 16) = wo2a; *(LAS u32x4*)(smem + L_WO2 + 5120 + w * 1024 + l * 16) = wo2b; }
    STAMP(8);
    WAITL_BAR();
    if (pass == 0 && __builtin_amdgcn_readfirstlane(*(volatile LAS unsigned*)(smem + L_FLAG)) == 0x7FA5C3E1u) { pass = 1; WAITV(0); continue; }
    WAITV(0);
    if (l == 0) publish(ws, F_O + (qblk * 8 + qg * 2 + kh) * 32 + head, tag);
    break;
  }
}

template <int NT> DI void zero_acc(f32x4 (&acc)[2][NT]) {
#pragma unroll
  for (int m = 0; m < 2; ++m)
#pragma unroll
    for (int t = 0; t < NT; ++t) { acc[m][t] = (f32x4){}; asm volatile("" : "+v"(acc[m][t])); }
  asm volatile("s_nop 1"); SBAR();
}
constexpr int wnext(int s, int W0, int W1, int N) { const int a = W0 + 2 * s, b = W1 + s, c = a < b ? a : b; return c < N ? c : N; }
constexpr int behind(int s, int upto, unsigned long long split, int extra) { int n = extra; for (int z = s + 1; z < upto; ++z) n += ((split >> z) & 1) ? 4 : 2; return n; }
template <int... I, class F> DI void sfor(std::integer_sequence<int, I...>, F f) { (f(std::integral_constant<int, I>{}), ...); }
template <int NT> struct BFrag { s16x4 v[NT][2]; };
template <int NT, int VM> DI void mstep(f32x4 (&acc)[2][NT], const BFrag<NT>& c, s16x8 a0, s16x8 a1, BFrag<NT>& n, LAS const char* nx, LAS const char* nx2, bool rd) {
  constexpr int H = (NT + 1) / 2;
#pragma unroll
  for (int t = 0; t < NT; ++t) {
    if (t == 0) { wait_vl<VM, 2 * (NT - H)>(); SBAR(); }
    if (t == H) { if (rd) LGKM(2 * H); else LGKM(0); SBAR(); }
    const s16x8 bb = cat(c.v[t][0], c.v[t][1]);
    mfma16i(acc[0][t], bb, a0); SBAR();
    if (rd) n.v[t][0] = ldstr(nx + t * 32); SBAR();
    mfma16i(acc[1][t], bb, a1); SBAR();
    if (rd) n.v[t][1] = ldstr(nx2 + t * 32); SBAR();
  }
}
template <int VM> DI void mstep5(f32x4 (&acc)[2][5], const s16x8 (&c)[5], s16x8 a0, s16x8 a1, s16x8 (&n)[5], LAS const char* nx, bool rd) {
#pragma unroll
  for (int t = 0; t < 5; ++t) {
    if (t == 0) { wait_vl<VM, 2>(); SBAR(); }
    if (t == 3) { if (rd) LGKM(3); else LGKM(0); SBAR(); }
    mfma16i(acc[0][t], c[t], a0); SBAR();
    if (rd) n[t] = ldsr128(nx + t * 1024); SBAR();
    mfma16i(acc[1][t], c[t], a1); SBAR();
  }
}
template <int NT> DI void rdfrag(BFrag<NT>& n, LAS const char* nx, LAS const char* nx2) {
#pragma unroll
  for (int t = 0; t < NT; ++t) { n.v[t][0] = ldstr(nx + t * 32); n.v[t][1] = ldstr(nx2 + t * 32); }
}
DI int wlane(int l, int rowb) { return (4 * (l >> 4) + ((l & 15) >> 2)) * rowb + (l & 3) * 8; }
DI void exchange5(f32x4 (&acc)[2][5], LAS char* buf, int w, int l) {
#pragma unroll
  for (int t = 0; t < 5; ++t) *(LAS f32x4*)(buf + w * 5120 + t * 1024 + l * 16) = acc[1][t];
  WAITL_BAR();
#pragma unroll
  for (int t = 0; t < 5; ++t) acc[0][t] += *(LAS const f32x4*)(buf + (w ^ 1) * 5120 + t * 1024 + l * 16);
}
template <int NT, u64 SPLIT> DI void store_frags(rsrc_t rs, unsigned fb, int t0, int nvalid, const u32x2 (&v)[NT], int l) {
  auto half = [&](int t) { __builtin_amdgcn_raw_buffer_store_b64(v[t], rs, l * 8, fb + (t0 + t) * 512, SC1); };
  auto pair = [&](int t) {
    if ((SPLIT >> ((t0 + t) >> 1)) & 1) { half(t); half(t + 1); }
    else __builtin_amdgcn_raw_buffer_store_b128((u32x4){v[t][0], v[t][1], v[t + 1][0], v[t + 1][1]}, rs, fb + (t0 + t) * 512 + l * 16, 0, SC1);
  };
  if (t0 & 1) {
    half(0);
#pragma unroll
    for (int t = 1; t + 1 < NT; t += 2) { if (t + 1 < nvalid) pair(t); else half(t); }
  } else {
#pragma unroll
    for (int t = 0; t + 1 < NT; t += 2) pair(t);
    if (NT - 1 < nvalid) half(NT - 1);
  }
}
constexpr u64 SPLIT_H = (1ull << 2) | (1ull << 7) | (1ull << 12) | (1ull << 17);
constexpr u64 SPLIT_U = (1ull << 3) | (1ull << 10) | (1ull << 13) | (1ull << 20) | (1ull << 30) | (1ull << 37) | (1ull << 40) | (1ull << 47);
DI s16x8 ldsplit(rsrc_t rs, unsigned voff8, unsigned soff) {
  return cat(__builtin_bit_cast(s16x4, __builtin_amdgcn_raw_buffer_load_b64(rs, voff8, soff, SC1)), __builtin_bit_cast(s16x4, __builtin_amdgcn_raw_buffer_load_b64(rs, voff8 + 512, soff, SC1))); }

__global__ void __launch_bounds__(512, 2) block_fwd(const char* x, const char* wqkv, const char* wo, const char* w1, const char* w2, char* ws, char* out) {
  extern __shared__ __attribute__((aligned(16))) char smem_[];
  LAS char* smem = (LAS char*)smem_;
  const unsigned lds0 = (unsigned)(size_t)smem;
  const int b = blockIdx.x, xcd = b & 7, bi = b >> 3;
  const int w = __builtin_amdgcn_readfirstlane(threadIdx.x >> 6);
  int l = threadIdx.x & 63;
  u64 pc; asm volatile("s_getpc_b64 %0" : "=s"(pc));
  STAMP(0);

  const rsrc_t rs = mkrs(ws, WS_END);

  u64 tag; int u1;
  {
    const int mb = (xcd >> 1) * 4 + (bi & 3), nb = (xcd & 1) * 8 + (bi >> 2);
    if (__builtin_expect(w >= 4 && nb != 15, 1)) tag = p1_loader(x, wqkv, ws, smem, mb, nb, w - 4, l STAMP_PASS);
    else {
  if (w == 3) {
    const rsrc_t rc = mkrs((const void*)(pc & ~255ull), CODE_PF);
    const u32x4 c0 = __builtin_amdgcn_raw_buffer_load_b128(rc, l * 16, bi * 1024, 0), c1 = __builtin_amdgcn_raw_buffer_load_b128(rc, l * 16, 32768 + bi * 1024, 0);
    asm volatile("" :: "v"(c0), "v"(c1));
  }
      const RowLoad rl = stats_load(x, ws, mb, nb, w, l);
      if (nb == 15) { tag = mktag((u64)rl.ep[1] << 32 | rl.ep[0]); p1_side(w1, ws, smem, mb, w, l STAMP_PASS); }
      else if (nb < 10) tag = p1_consumer<false>(rl, ws, smem, mb, nb, w, l STAMP_PASS);
      else tag = p1_consumer<true>(rl, ws, smem, mb, nb, w, l STAMP_PASS);
    }
    STAMP(3);
    u1 = nb * 16 + mb;
  }
  const int mb64 = xcd * 4 + (bi >> 3), nb80 = bi & 7;
  const int wm = (w >> 1) & 1, wk = w & 1;
  const int mt0 = mb64 * 4 + wm * 2 + wk;
  const int mb128 = xcd * 2 + (bi >> 4), j8 = bi & 7;
  const int t4 = ((bi >> 3) & 1) * 54 + 7 * j8 - (j8 >> 2), n4 = (j8 & 3) == 3 ? 6 : 7;
  u32x4 w1r[15];
  p2_attention(wo, w1, w2, ws, smem, w, u1, w1r, tag, pc STAMP_PASS);
  l = lane_again();
  const int g = l >> 4;
  auto w1_put = [&](int n0, int n1) {
#pragma unroll
    for (int n = n0; n < n1; ++n) *(LAS u32x4*)(smem + L_W1 + w1_slot(n) * 7168 + w * 1024 + l * 16) = w1r[n];
  };
  u32x2 hres[5];
  STAMP(9);
  if (w < 4) {
    const int q4 = (l & 15) >> 2, pc = (l & 3) * 8;
    const int rm = wk * 40 + (g >> 1) * 20 + (g & 1) * 4 + q4, rr = wk * 160 + g * 20 + 16 + q4;
    auto at = [&](int row) { return smem + (row < 576 ? L_WO + row * 160 : L_WO2 + (row - 576) * 160) + pc; };
    LAS const char* const wb[6] = {opaque(smem + L_WO + rm * 160 + pc), opaque(smem + L_WO + (rm + 320) * 160 + pc), opaque(at(rm + 560)),
                                   opaque(smem + L_WO + rr * 160 + pc), opaque(smem + L_WO + (rr + 320) * 160 + pc), opaque(at(rr + 400))};
    auto w0 = [&](int s) { return s < 7 ? wb[s >> 2] + (s & 3) * 12800 : s == 7 ? wb[2] : wb[s - 5]; };
    auto w1_ = [&](int s) { return s < 8 ? w0(s) + 1280 : s == 8 ? wb[3] + 12800 : wb[5]; };
    f32x4 acc[2][5]; zero_acc<5>(acc);
    BFrag<5> bf[2];
    rdfrag<5>(bf[0], w0(0), w1_(0));
    if (PD2) __builtin_amdgcn_s_sleep(PD2);
    { const int idx[1] = {F_O + (mb64 * 2 + wm) * 32 + (l & 31)}; await<1>(ws, idx, tag); }
    STAMP(10);
    s16x8 a[10][2];
    auto lda = [&](int s) {
#pragma unroll
      for (int m = 0; m < 2; ++m) {
        const unsigned o = WS_O + (mt0 ^ m) * 20480;
        if (s < 8) a[s][m] = ldfrag(rs, l * 16, o + (2 * s + wk) * 1024);
        else a[s][m] = ldsplit(rs, l * 8, o + 16384 + (2 * (s - 8) + wk) * 1024);
      }
      SBAR();
    };
#pragma unroll
    for (int s = 0; s < WIN3; ++s) lda(s);
    u32x2 xr[5];
    sfor(std::make_integer_sequence<int, 10>{}, [&](auto S) {
      constexpr int s = S;
      if (s + WIN3 < 10) lda(s + WIN3);
      if (s + WIN3 == 10) {
#pragma unroll
        for (int t = 0; t < 5; ++t) xr[t] = *(const u32x2*)(x + (mt0 * 16 + (l & 15)) * 1280 + nb80 * 160 + t * 32 + g * 8);
        BAR();
      }
      mstep<5, behind(s, s + WIN3 < 10 ? s + WIN3 + 1 : 10, 0x300, s + WIN3 >= 10 ? 5 : 0)>(acc, bf[s & 1], a[s][0], a[s][1], bf[(s & 1) ^ 1], w0(s < 9 ? s + 1 : 9), w1_(s < 9 ? s + 1 : 9), s < 9);
    });
    MFMA_DONE();
    STAMP(11);
    exchange5(acc, smem, w, l);
    STAMP(19);
    float s1 = 0.f, s2 = 0.f;
#pragma unroll
    for (int t = 0; t < 5; ++t) {
      hres[t] = (u32x2){pk(acc[0][t][0] + bflo(xr[t][0]), acc[0][t][1] + bfhi(xr[t][0])), pk(acc[0][t][2] + bflo(xr[t][1]), acc[0][t][3] + bfhi(xr[t][1]))};
#pragma unroll
      for (int e = 0; e < 2; ++e) { const float p = bflo(hres[t][e]), q = bfhi(hres[t][e]); s1 += p + q; s2 = fmaf(p, p, fmaf(q, q, s2)); }
    }
    store_frags<5, SPLIT_H>(rs, WS_H + mt0 * 20480, nb80 * 5, 5, hres, l);
    s1 = sum32(sum16(s1)); s2 = sum32(sum16(s2));
    if (l < 16) __builtin_amdgcn_raw_buffer_store_b64((u32x2){__float_as_uint(s1), __float_as_uint(s2)}, rs, WS_ST + mt0 * 1024 + nb80 * 128 + l * 8, 0, SC1);
    BAR();
    STAMP(20);
    SBAR(); w1_put(0, W1_PRE);
    WAITV(0);
    if (l == 0) publish(ws, F_H + (mb64 * 2 + wm) * 16 + nb80 * 2 + wk, tag);
    STAMP(21);
    SBAR(); w1_put(W1_PRE, 15);
    STAMP(12);
    WAITL_BAR();
    if (PD3) __builtin_amdgcn_s_sleep(PD3);
    { const int idx[1] = {(CSF && (l & 16)) ? F_QKV + 240 + (l & 15) : F_H + (mb128 * 4 + w) * 16 + (l & 15)}; await<1>(ws, idx, tag); }
    STAMP(13);
    {
      const int mtb = mb128 * 8 + w * 2;
      f32x4 stv[2]; f32x4 cs[7];
#pragma unroll
      for (int m = 0; m < 2; ++m) stv[m] = __builtin_bit_cast(f32x4, ldsplit(rs, l * 8, WS_ST + (mtb + m) * 1024));
      constexpr int WIN = WIN4;
      s16x8 ah[20][2];
      unsigned vh[2] = {l * 16u, l * 16u + 20480}, vh8[2] = {l * 8u, l * 8u + 20480}; asm volatile("" : "+v"(vh[0]), "+v"(vh[1]), "+v"(vh8[0]), "+v"(vh8[1]));
      auto ldh = [&](int s) {
        const unsigned o = WS_H + (mtb * 20 + (s & ~3)) * 1024;
#pragma unroll
        for (int m = 0; m < 2; ++m) ah[s][m] = ((SPLIT_H >> s) & 1) ? ldsplit(rs, vh8[m] + (s & 3) * 1024, o) : ldfrag(rs, vh[m] + (s & 3) * 1024, o);
        SBAR();
      };
#pragma unroll
      for (int s = 0; s < WIN; ++s) ldh(s);
      SBAR();
      f32x4 acc4[2][7]; zero_acc<7>(acc4);
      BFrag<7> b7[2];
      LAS const char* wq = smem + L_W1 + wlane(l, 224);
      LAS const char* const wb4[3] = {opaque(wq), opaque(wq + 57344), opaque(wq + 114688)};
      rdfrag<7>(b7[0], wb4[0], wb4[0] + 3584);
      sfor(std::make_integer_sequence<int, 20>{}, [&](auto S) {
        constexpr int s = S;
#pragma unroll
        for (int z = wnext(s, WIN, WMAX4, 20); z < wnext(s + 1, WIN, WMAX4, 20); ++z) ldh(z);
        if (s == 20 - WMAX4) {
#pragma unroll
          for (int t = 0; t < 7; ++t) cs[t] = __builtin_bit_cast(f32x4, __builtin_amdgcn_raw_buffer_load_b128(rs, g * 16 + (t & 3) * 64, WS_CS + (t4 + (t & 4)) * 64, SC1));
          BAR();
        }
        LAS const char* nx = wb4[((s + 1) >> 3) % 3] + ((s + 1) & 7) * 7168;
        mstep<7, behind(s, wnext(s + 1, WIN, WMAX4, 20), SPLIT_H, s >= 20 - WMAX4 ? 7 : 0)>(acc4, b7[s & 1], ah[s][0], ah[s][1], b7[(s & 1) ^ 1], nx, nx + 3584, s < 19);
      });
      MFMA_DONE();
      STAMP(14);
#pragma unroll
      for (int m = 0; m < 2; ++m) {
        const float t1 = sum32(sum16(stv[m][0] + stv[m][2])), t2 = sum32(sum16(stv[m][1] + stv[m][3]));
        const float mu = t1 * (1.f / DM), rsd = __builtin_amdgcn_rsqf(fmaxf(t2 * (1.f / DM) - mu * mu, 0.f) + LN_EPS);
        u32x2 uv[7];
#pragma unroll
        for (int t = 0; t < 7; ++t) {
          float v[4];
#pragma unroll
          for (int e = 0; e < 4; ++e) v[e] = (acc4[m][t][e] - mu * cs[t][e]) * rsd;
          uv[t] = (u32x2){relu2(pk(v[0], v[1])), relu2(pk(v[2], v[3]))};
        }
        store_frags<7, SPLIT_U>(rs, WS_U + (mtb + m) * 55296, t4, n4, uv, l);
      }
      BAR();
      WAITV(0);
      if (l == 0) publish(ws, F_U + (mb128 * 4 + w) * 16 + (bi & 15), tag);
      STAMP(15);
      if (PD4) __builtin_amdgcn_s_sleep(PD4);
      { const int idx[1] = {F_U + (mb64 * 2 + wm) * 16 + (l & 15)}; await<1>(ws, idx, tag); }
      STAMP(16);
    }
    {
      constexpr int WIN = WIN5;
      s16x8 au[27][2];
      unsigned vu[2] = {l * 16u + wk * 55296, l * 16u + (wk ^ 1) * 55296}, vu8[2] = {l * 8u + wk * 55296, l * 8u + (wk ^ 1) * 55296}; asm volatile("" : "+v"(vu[0]), "+v"(vu[1]), "+v"(vu8[0]), "+v"(vu8[1]));
      auto ldu = [&](int s) {
        const bool plain = uperm(2 * s) == 2 * s;
        const unsigned o = WS_U + ((mt0 & ~1) * 54 + (plain ? 2 * (s & ~1) + wk : wk ? 2 * s + 1 : uperm(2 * s))) * 1024, i = plain ? (s & 1) * 2048 : 0;
#pragma unroll
        for (int m = 0; m < 2; ++m) au[s][m] = ((SPLIT_U >> (2 * s + 1)) & 1) ? ldsplit(rs, vu8[m], o) : ldfrag(rs, vu[m] + i, o);
        SBAR();
      };
#pragma unroll
      for (int s = 0; s < WIN; ++s) ldu(s);
      SBAR();
      f32x4 acc5[2][5]; zero_acc<5>(acc5);
      s16x8 b5[2][5];
      LAS const char* wq = smem + l * 16 + wk * 5120;
      LAS const char* const wb5[3] = {opaque(wq), opaque(wq + 61440), opaque(wq + 122880)};
      BAR();
#pragma unroll
      for (int t = 0; t < 5; ++t) b5[0][t] = ldsr128(wb5[0] + t * 1024);
      sfor(std::make_integer_sequence<int, 27>{}, [&](auto S) {
        constexpr int s = S;
#pragma unroll
        for (int z = wnext(s, WIN, WMAX5, 27); z < wnext(s + 1, WIN, WMAX5, 27); ++z) ldu(z);
        if ((s & 3) == 3) WAITL_BAR();
        mstep5<behind(s, wnext(s + 1, WIN, WMAX5, 27), 0x840042, 0)>(acc5, b5[s & 1], au[s][0], au[s][1], b5[(s & 1) ^ 1],
                                                                     wb5[(((s + 1) >> 1) & 7) / 3] + (((s + 1) >> 1) & 7) % 3 * 20480 + ((s + 1) & 1) * 10240, s < 26);
      });
      MFMA_DONE();
      STAMP(17);
      exchange5(acc5, smem, w, l);
#pragma unroll
      for (int t = 0; t < 5; ++t) {
        const u32x2 o = {pk(acc5[0][t][0] + bflo(hres[t][0]), acc5[0][t][1] + bfhi(hres[t][0])), pk(acc5[0][t][2] + bflo(hres[t][1]), acc5[0][t][3] + bfhi(hres[t][1]))};
        *(u32x2*)(out + (mt0 * 16 + (l & 15)) * 1280 + nb80 * 160 + t * 32 + g * 8) = o;
      }
    }
  } else {
    const int lw = w - 4;
    const rsrc_t r1 = mkrs(w1, DM * DFF * 2), r2 = mkrs(ws + WS_W2 + nb80 * 276480, 276480);
    u32x4 w2r[NW];
    auto w2_get = [&](int n0, int n1) {
#pragma unroll
      for (int n = n0; n < n1; ++n) {
        w2r[n % NW] = __builtin_amdgcn_raw_buffer_load_b128(r2, l * 16 + lw * 5120 + (n % 5) * 1024, (n / 5) * 20480, SC1);
        SBAR();
      }
    };
    auto w2_put = [&](int n0, int n1) {
#pragma unroll
      for (int n = n0; n < n1; ++n) *(LAS u32x4*)(smem + ((n / 5) & 7) * 20480 + lw * 5120 + (n % 5) * 1024 + l * 16) = w2r[n % NW];
    };
    { const int idx[4] = {l, 64 + l, 128 + l, 192 + l}; await<4, 8, WS_FW2>(ws, idx, tag); }
    if (b == 0 && w == 7 && l == 0)
      __hip_atomic_store((GAS u64*)(ws + WS_EPOCH), tag, __ATOMIC_RELAXED, __HIP_MEMORY_SCOPE_AGENT);
    BAR();
#pragma unroll 1
    for (int n = lw; n < NOW1E; n += 4) { const int i = n / 7, p = n - i * 7; dma16(r1, lds0 + L_W1 + (i + 2) * 7168 + p * 1024, w1_lane(p, l, t4), (i + 2) * (32 * 3456)); }
    w2_get(0, N1);
    BAR();
    if (lw < 3) w1_put(0, 15);
    BAR();
    w2_get(N1, NH);
    asm volatile("s_waitcnt vmcnt(%0) lgkmcnt(0)\n\ts_barrier" :: "n"(NH) : "memory");
    BAR();
    w2_get(NH, N2);
    BAR();
    w2_put(0, 14);
    w2_get(N2, NU);
    w2_put(14, 40);
    WAITL_BAR();
    BAR(); w2_put(40, 50);
    BAR(); w2_put(50, NU < 60 ? NU : 60);
    BAR(); w2_put(60, NU < 60 ? 60 : NU);
    WAITL_BAR();
    w2_get(NU, 70);
    w2_put(NU, NU < 60 ? 60 : NU);
    WAITL_BAR();
    w2_put(NU < 60 ? 60 : NU, 70);
    WAITL_BAR();
    BAR();
  }
}

extern "C" void kernel_launch(void* const* d_in, const int* in_sizes, int n_in, void* d_out, int out_size, void* d_ws, size_t ws_size, hipStream_t stream) {
  (void)hipFuncSetAttribute((const void*)block_fwd, hipFuncAttributeMaxDynamicSharedMemorySize, LDS_BYTES);
  block_fwd<<<256, 512, LDS_BYTES, stream>>>((const char*)d_in[0], (const char*)d_in[1], (const char*)d_in[2], (const char*)d_in[4], (const char*)d_in[5], (char*)d_ws + WS_AT, (char*)d_out);
}
```
